```python
import torch

def _softmax(x, axis=-1):
    return torch.softmax(x, dim=axis)
def _gelu(x):
    return torch.nn.functional.gelu(x, approximate="tanh")
def _silu(x):
    return torch.nn.functional.silu(x)
def _sigmoid(x):
    return torch.sigmoid(x)
def _layernorm(x, axis=-1, eps=1e-5):
    m = x.mean(dim=axis, keepdim=True)
    v = x.var(dim=axis, keepdim=True, unbiased=False)
    return (x - m) / torch.sqrt(v + eps)
def _scatter_add(tgt, idx, src, axis=0):
    return tgt.clone().index_add_(axis, idx, src)
def _segment_sum(src, seg, n):
    out = torch.zeros((n,) + src.shape[1:], dtype=src.dtype)
    return out.index_add_(0, seg, src)
def _pad(x, pw):
    flat = tuple(v for pair in reversed(pw) for v in pair)
    return torch.nn.functional.pad(x, flat)

kernel_name = 'gated_mix__sliding_block__dense_activation_silu__prompt'

def setup_inputs():

    _w = (lambda a: (lambda t: t.to(torch.bfloat16) if t.is_floating_point() else t)(torch.as_tensor(a)))
    def _A_setup():
        B, S, D = 1, 2048, 576
        return {
            "x": _w((lambda *s: torch.randn(*s, dtype=torch.bfloat16) * 0.2)(B, S, D)),
            "wqkv": _w((lambda *s: torch.randn(*s, dtype=torch.bfloat16) * 0.2)(D, 3 * D)),
            "wo": _w((lambda *s: torch.randn(*s, dtype=torch.bfloat16) * 0.2)(D, D)),
        }
    _A_in = _A_setup()

    def _B_setup():
        B, S, D, F = 1, 2048, 576, 4608
        return {
            "x": _w((lambda *s: torch.randn(*s, dtype=torch.bfloat16) * 0.2)(B, S, D)),
            "w1": _w((lambda *s: torch.randn(*s, dtype=torch.bfloat16) * 3.2)(D, F) * (D ** -0.5)),
            "w2": _w((lambda *s: torch.randn(*s, dtype=torch.bfloat16) * 3.2)(F, D) * (F ** -0.5)),
        }
    _B_in = _B_setup()

    return {
        "A_x": _A_in["x"],
        "A_wqkv": _A_in["wqkv"],
        "A_wo": _A_in["wo"],
        "B_x": _B_in["x"],
        "B_w1": _B_in["w1"],
        "B_w2": _B_in["w2"],
        "gate_w": _w((lambda *s: torch.randn(*s, dtype=torch.bfloat16) * 0.2)(576) * 16.0),
    }

def reference(A_x, A_wqkv, A_wo, B_x, B_w1, B_w2, gate_w):
    def _A_ref(x, wqkv, wo):
        B, S, D = x.shape
        H = 32; Dh = D // H; W = 256
        qkv = (lambda x, *d: x.permute(*d))((x @ wqkv).reshape(B, S, 3, H, Dh), 2, 0, 3, 1, 4)
        q, k, v = qkv[0], qkv[1], qkv[2]
        scores = torch.einsum("bhqd,bhkd->bhqk", q, k) * (Dh ** -0.5)
        idx = torch.arange(S)
        mask = (idx[:, None] - idx[None, :] >= 0) & (idx[:, None] - idx[None, :] < W)
        scores = torch.where(mask[None, None], scores, -1e9)
        p = _softmax(scores, axis=-1)
        o = torch.einsum("bhqk,bhkd->bhqd", p, v)
        return (lambda x, *d: x.permute(*d))(o, 0, 2, 1, 3).reshape(B, S, D) @ wo

    def _B_ref(x, w1, w2):
        h = torch.einsum("bsd,df->bsf", x, w1)
        h = _silu(h)
        out = torch.einsum("bsf,fd->bsd", h, w2)
        return out

    x = A_x
    g = _sigmoid((x * gate_w).sum(-1, keepdims=True))
    a = _A_ref(x, A_wqkv, A_wo)
    b = _B_ref(x, B_w1, B_w2)
    return g * a + (1 - g) * b
```

```cpp
#include <hip/hip_runtime.h>
#include <hip/hip_bf16.h>
typedef __hip_bfloat16 bf16;
static __device__ __forceinline__ float b2f(bf16 x){ return __bfloat162float(x);}
static __device__ __forceinline__ bf16 f2b(float x){ return __float2bfloat16(x);}
__global__ void gemm_naive(const bf16* A, const bf16* B, bf16* C, int M, int N, int K, int mode){
  __shared__ float As[16][17], Bs[16][17];
  int tx=threadIdx.x, ty=threadIdx.y;
  int row=blockIdx.y*16+ty, col=blockIdx.x*16+tx;
  float acc=0;
  for(int k0=0;k0<K;k0+=16){
    As[ty][tx]=b2f(A[row*K+k0+tx]);
    Bs[ty][tx]=b2f(B[(k0+ty)*N+col]);
    __syncthreads();
    for(int k=0;k<16;k++) acc+=As[ty][k]*Bs[k][tx];
    __syncthreads();
  }
  if(mode==1){ float h=b2f(f2b(acc)); acc=h/(1.f+__expf(-h)); }
  C[row*N+col]=f2b(acc);
}
__global__ void attn_naive(const bf16* qkv, bf16* o){
  int i=blockIdx.x*64+threadIdx.x; int h=blockIdx.y;
  float q[18]; for(int d=0;d<18;d++) q[d]=b2f(qkv[i*1728+h*18+d]);
  int j0=i-255; if(j0<0) j0=0;
  float m=-1e30f;
  for(int j=j0;j<=i;j++){ float s=0; for(int d=0;d<18;d++) s+=q[d]*b2f(qkv[j*1728+576+h*18+d]); s*=0.23570226f; m=fmaxf(m,s);}
  float l=0; float acc[18]; for(int d=0;d<18;d++) acc[d]=0;
  for(int j=j0;j<=i;j++){ float s=0; for(int d=0;d<18;d++) s+=q[d]*b2f(qkv[j*1728+576+h*18+d]); s*=0.23570226f; float p=__expf(s-m); l+=p; for(int d=0;d<18;d++) acc[d]+=p*b2f(qkv[j*1728+1152+h*18+d]);}
  for(int d=0;d<18;d++) o[i*576+h*18+d]=f2b(acc[d]/l);
}
__global__ void combine(const bf16* x, const bf16* gw, const bf16* a, const bf16* b, bf16* out){
  int row=blockIdx.x; int t=threadIdx.x;
  float s=0; for(int d=t;d<576;d+=64) s+=b2f(f2b(b2f(x[row*576+d])*b2f(gw[d])));
  for(int o=32;o>0;o>>=1) s+=__shfl_xor(s,o);
  s=b2f(f2b(s));
  float g=b2f(f2b(1.f/(1.f+__expf(-s))));
  float g1=b2f(f2b(1.f-g));
  for(int d=t;d<576;d+=64){ float v=b2f(f2b(g*b2f(a[row*576+d])))+b2f(f2b(g1*b2f(b[row*576+d]))); out[row*576+d]=f2b(v);}
}
extern "C" void kernel_launch(void* const* d_in, const int* in_sizes, int n_in, void* d_out, int out_size, void* d_ws, size_t ws_size, hipStream_t stream){
  const bf16* x=(const bf16*)d_in[0]; const bf16* wqkv=(const bf16*)d_in[1]; const bf16* wo=(const bf16*)d_in[2];
  const bf16* w1=(const bf16*)d_in[4]; const bf16* w2=(const bf16*)d_in[5]; const bf16* gw=(const bf16*)d_in[6];
  bf16* ws=(bf16*)d_ws; bf16* qkv=ws; bf16* o=qkv+2048*1728; bf16* a=o+2048*576; bf16* h=a+2048*576; bf16* b=h+2048*4608;
  dim3 bl(16,16);
  gemm_naive<<<dim3(1728/16,128),bl,0,stream>>>(x,wqkv,qkv,2048,1728,576,0);
  attn_naive<<<dim3(32,32),64,0,stream>>>(qkv,o);
  gemm_naive<<<dim3(576/16,128),bl,0,stream>>>(o,wo,a,2048,576,576,0);
  gemm_naive<<<dim3(4608/16,128),bl,0,stream>>>(x,w1,h,2048,4608,576,1);
  gemm_naive<<<dim3(576/16,128),bl,0,stream>>>(h,w2,b,2048,576,4608,0);
  combine<<<2048,64,0,stream>>>(x,gw,a,b,(bf16*)d_out);
}
```

```cpp
#include <hip/hip_runtime.h>
__global__ __launch_bounds__(512, 2) void fused_k(const void* x, const void* wqkv, const void* wo, const void* w1, const void* w2,
                                                  const void* gw, void* out, void* ws) {
#if defined(__HIP_DEVICE_COMPILE__)
  unsigned tid = threadIdx.x;
  asm volatile(
"s_load_dwordx16 s[4:19], %1, 0\n"
"s_getpc_b64 s[60:61]\n"
"LPC_%=:\n"
"s_and_b32 s62, %2, 0xf8\n"
"v_lshrrev_b32 v128, 5, v0\n"
"v_and_or_b32 v128, v0, 1, v128\n"
"v_lshl_add_u32 v128, s62, 1, v128\n"
"v_lshlrev_b32 v128, 7, v128\n"
"v_min_u32 v128, LEN_%=-LPC_%=-4, v128\n"
"s_mov_b64 exec, 3\n"
"global_load_dword v239, v128, s[60:61]\n"
"s_mov_b64 exec, -1\n"
"v_lshrrev_b32 v128, 6, v0\n"
"v_and_b32 v140, 63, v0\n"
"s_and_b32 s60, %2, 7\n"
"v_readfirstlane_b32 s46, v128\n"
"s_lshr_b32 s61, %2, 3\n"
"s_and_b32 s33, s61, 15\n"
"s_and_b32 s62, s60, 1\n"
"s_lshl_b32 s62, s62, 4\n"
"s_add_u32 s47, s62, s33\n"
"s_lshr_b32 s60, s60, 1\n"
"s_lshr_b32 s61, s61, 4\n"
"s_lshl2_add_u32 s48, s61, s60\n"
"s_and_b32 s34, s47, 1\n"
"s_lshl_b32 s61, s61, 1\n"
"s_add_u32 s61, s61, s34\n"
"s_lshl_b32 s61, s61, 1\n"
"s_lshr_b32 s60, s33, 1\n"
"s_add_u32 s65, s60, s61\n"
"s_sub_u32 s60, s65, 9\n"
"s_cselect_b32 s65, s65, s60\n"
"s_and_b32 s49, s46, 3\n"
"s_lshl_b32 s60, s48, 2\n"
"s_mul_i32 s61, s48, 3\n"
"s_add_u32 s61, s61, 3\n"
"s_lshl_b32 s62, s34, 1\n"
"s_sub_u32 s63, 1, s62\n"
"s_lshl_b32 s64, s34, 5\n"
"s_cmp_lt_u32 s48, 3\n"
"s_cselect_b32 s66, s60, s61\n"
"s_cselect_b32 s67, 1, s63\n"
"s_cselect_b32 s35, 0, s64\n"
"s_add_u32 s66, s66, s62\n"
"s_lshl_b32 s40, s63, 7\n"
"v_lshrrev_b32 v129, 3, v140\n"
"v_and_b32 v130, 7, v140\n"
"v_lshlrev_b32 v255, 4, v140\n"
"s_cmp_ge_u32 s46, 4\n"
"s_cbranch_scc1 LH1_%=\n"
"s_lshl_b32 s60, s65, 1\n"
"v_add_u32 v240, s60, v140\n"
"v_subrev_u32 v241, 18, v240\n"
"v_min_u32 v240, v240, v241\n"
"v_mul_u32_u24 v241, 43, v140\n"
"v_lshrrev_b32 v241, 8, v241\n"
"v_mul_u32_u24 v241, 6, v241\n"
"v_sub_u32 v241, v140, v241\n"
"v_not_b32 v241, v241\n"
"v_and_b32 v241, 3, v241\n"
"v_lshlrev_b32 v241, 10, v241\n"
"s_mov_b32 s60, 110592\n"
"v_mad_u32_u24 v242, v240, s60, v241\n"
"s_and_b32 s62, s49, 1\n"
"s_lshl_b32 s62, s62, 2\n"
"v_and_or_b32 v128, v129, 2, s62\n"
"v_readlane_b32 s68, v242, 0\n"
"v_readlane_b32 s69, v242, 1\n"
"v_readlane_b32 s70, v242, 2\n"
"v_readlane_b32 s71, v242, 3\n"
"v_readlane_b32 s72, v242, 4\n"
"v_readlane_b32 s73, v242, 5\n"
"v_xor_b32 v128, v128, v130\n"
"v_lshlrev_b32 v128, 4, v128\n"
"s_lshl_b32 s63, s49, 3\n"
"v_add_u32 v129, s63, v129\n"
"s_movk_i32 s60, 3456\n"
"v_mad_u32_u24 v250, v129, s60, v128\n"
"s_lshl_b32 s60, s66, 7\n"
"v_add_u32 v250, s60, v250\n"
"s_mul_i32 s57, s49, 6144\n"
"s_add_u32 m0, s57, 98304\n"
"s_mov_b32 s27, 0x20000\n"
"s_waitcnt lgkmcnt(0)\n"
"s_sub_u32 s24, s6, 3072\n"
"s_subb_u32 s25, s7, 0\n"
"s_mov_b32 s26, 1993728\n"
"buffer_load_dwordx4 v250, s[24:27], s68 offen lds\n"
"buffer_load_dwordx4 v250, s[24:27], s69 offen offset:1024 lds\n"
"v_readlane_b32 s74, v242, 6\n"
"v_readlane_b32 s75, v242, 7\n"
"v_readlane_b32 s76, v242, 8\n"
"v_readlane_b32 s77, v242, 9\n"
"v_readlane_b32 s78, v242, 10\n"
"v_readlane_b32 s79, v242, 11\n"
"v_readlane_b32 s80, v242, 12\n"
"v_readlane_b32 s81, v242, 13\n"
"v_readlane_b32 s82, v242, 14\n"
"v_readlane_b32 s83, v242, 15\n"
"v_readlane_b32 s84, v242, 16\n"
"v_readlane_b32 s85, v242, 17\n"
"s_mov_b32 s60, 294912\n"
"v_mad_u32_u24 v251, v240, s60, v241\n"
"s_movk_i32 s60, 9216\n"
"v_mad_u32_u24 v189, v129, s60, v128\n"
"s_mul_i32 s60, s48, 1152\n"
"s_lshl_b32 s61, s34, 10\n"
"s_add_u32 s60, s60, s61\n"
"v_add_u32 v189, s60, v189\n"
"s_lshl_b32 s58, s67, 7\n"
"s_lshl_b32 s59, s49, 10\n"
"s_mov_b32 s39, 0x20000\n"
"s_branch LJ_%=\n"
"LH1_%=:\n"
"v_lshrrev_b32 v128, 4, v140\n"
"v_xor_b32 v128, v128, v130\n"
"v_lshlrev_b32 v128, 4, v128\n"
"s_lshr_b32 s60, s49, 1\n"
"s_xor_b32 s60, s60, s47\n"
"s_lshl_b32 s60, s60, 1\n"
"s_and_b32 s61, s49, 1\n"
"s_add_u32 s60, s60, s61\n"
"s_lshl_b32 s60, s60, 5\n"
"v_add_u32 v129, s60, v129\n"
"s_movk_i32 s60, 1152\n"
"v_mad_u32_u24 v136, v129, s60, v128\n"
"v_xor_b32 v137, 64, v136\n"
"v_add_u32 v137, 8192, v137\n"
"v_add_u32 v138, 16384, v136\n"
"v_add_u32 v139, 16384, v137\n"
"s_lshl_b32 s57, s49, 12\n"
"s_lshl_b32 s58, s65, 7\n"
"s_lshl_b32 s59, s49, 10\n"
"s_add_u32 m0, s57, 122880\n"
"s_mov_b32 s38, 2359296\n"
"s_mov_b32 s39, 0x20000\n"
"s_waitcnt lgkmcnt(0)\n"
"s_mov_b64 s[36:37], s[4:5]\n"
"buffer_load_dwordx4 v136, s[36:39], s58 offen lds\n"
"buffer_load_dwordx4 v137, s[36:39], s58 offen offset:1024 lds\n"
"buffer_load_dwordx4 v138, s[36:39], s58 offen offset:2048 lds\n"
"buffer_load_dwordx4 v139, s[36:39], s58 offen offset:3072 lds\n"
"s_add_u32 s58, s58, 128\n"
"s_cmp_eq_u32 s58, 1152\n"
"s_cselect_b32 s58, 0, s58\n"
"s_lshl_b32 s60, s49, 6\n"
"s_lshl_b32 s61, s65, 3\n"
"s_add_u32 s61, s61, s60\n"
"v_add_u32 v240, s61, v140\n"
"v_subrev_u32 v241, 72, v240\n"
"v_min_u32 v240, v240, v241\n"
"v_lshlrev_b32 v240, 4, v240\n"
"s_lshl_b32 s61, s33, 7\n"
"s_lshl_b32 s60, s49, 5\n"
"s_add_u32 s60, s60, s61\n"
"v_add_u32 v251, s60, v140\n"
"v_lshlrev_b32 v251, 7, v251\n"
"LJ_%=:\n"
"s_load_dwordx2 s[50:51], s[18:19], 0\n"
"v_lshrrev_b32 v128, 4, v140\n"
"v_and_b32 v129, 15, v140\n"
"v_lshrrev_b32 v130, 1, v129\n"
"v_xor_b32 v130, v130, v128\n"
"v_lshlrev_b32 v130, 4, v130\n"
"v_lshl_add_u32 v130, v129, 7, v130\n"
"s_lshr_b32 s60, s46, 1\n"
"s_lshl_b32 s60, s60, 12\n"
"s_add_u32 s61, s60, 49152\n"
"v_add_u32 v184, s61, v130\n"
"v_xor_b32 v185, 64, v184\n"
"v_add_u32 v186, 73728, v184\n"
"v_add_u32 v187, 73728, v185\n"
"v_lshrrev_b32 v130, 2, v129\n"
"v_mul_u32_u24 v131, 48, v128\n"
"v_add_u32 v131, v131, v130\n"
"v_and_b32 v130, 2, v130\n"
"v_and_b32 v128, 1, v128\n"
"v_lshl_or_b32 v130, v128, 2, v130\n"
"v_bfe_u32 v128, v140, 1, 1\n"
"s_and_b32 s61, s49, 1\n"
"s_lshl_b32 s61, s61, 2\n"
"v_or_b32 v128, s61, v128\n"
"v_xor_b32 v130, v130, v128\n"
"v_lshlrev_b32 v130, 4, v130\n"
"v_lshl_add_u32 v130, v131, 7, v130\n"
"v_and_b32 v128, 1, v140\n"
"v_lshl_add_u32 v252, v128, 3, v130\n"
"v_xor_b32 v188, 32, v252\n"
"v_add_u32 v253, 98304, v252\n"
"v_add_u32 v254, 98304, v188\n"
"s_mov_b64 s[28:29], s[18:19]\n"
"s_mov_b32 s30, 36241408\n"
"s_mov_b32 s31, 0x20000\n"
"s_lshr_b32 s62, s46, 2\n"
"s_xor_b32 s62, s47, s62\n"
"v_lshrrev_b32 v128, 5, v140\n"
"s_and_b32 s60, s49, 1\n"
"s_lshl_b32 s60, s60, 2\n"
"v_add_u32 v128, s60, v128\n"
"v_and_b32 v129, 0x1f0, v255\n"
"s_mul_i32 s60, s66, 57\n"
"s_lshr_b32 s60, s60, 9\n"
"s_mul_i32 s61, s60, 9\n"
"s_sub_u32 s61, s66, s61\n"
"s_lshl_b32 s61, s61, 3\n"
"s_sub_u32 s63, 31, s62\n"
"s_mul_i32 s63, s63, 19456\n"
"s_mul_i32 s64, s60, 9216\n"
"s_add_u32 s63, s63, s64\n"
"s_add_u32 s63, s63, 2415616\n"
"s_mul_i32 s64, s62, 9216\n"
"s_add_u32 s64, s64, 65536\n"
"s_cmp_eq_u32 s60, 0\n"
"s_cselect_b32 s52, s64, s63\n"
"s_cselect_b32 s54, 0x3eae1a93, 1.0\n"
"s_mov_b32 s63, 622592\n"
"s_cselect_b32 s63, 294912, s63\n"
"s_lshr_b32 s64, s49, 1\n"
"s_lshl_b32 s64, s64, 9\n"
"s_add_u32 s52, s52, s64\n"
"s_cmp_eq_u32 s60, 2\n"
"s_cselect_b32 s64, 3, 0\n"
"v_add3_u32 v130, v128, s61, 0\n"
"v_mul_u32_u24 v131, 57, v130\n"
"v_lshrrev_b32 v131, 9, v131\n"
"v_mul_u32_u24 v245, 9, v131\n"
"v_sub_u32 v130, v130, v245\n"
"v_mul_u32_u24 v131, s63, v131\n"
"v_lshl_add_u32 v131, v130, 10, v131\n"
"v_and_b32 v130, s64, v130\n"
"v_lshlrev_b32 v130, 6, v130\n"
"v_xor_b32 v130, v130, v129\n"
"v_add_u32 v245, v131, v130\n"
"v_add3_u32 v130, v128, s61, 2\n"
"v_mul_u32_u24 v131, 57, v130\n"
"v_lshrrev_b32 v131, 9, v131\n"
"v_mul_u32_u24 v246, 9, v131\n"
"v_sub_u32 v130, v130, v246\n"
"v_mul_u32_u24 v131, s63, v131\n"
"v_lshl_add_u32 v131, v130, 10, v131\n"
"v_and_b32 v130, s64, v130\n"
"v_lshlrev_b32 v130, 6, v130\n"
"v_xor_b32 v130, v130, v129\n"
"v_add_u32 v246, v131, v130\n"
"s_add_u32 s66, s66, s67\n"
"s_mul_i32 s60, s66, 57\n"
"s_lshr_b32 s60, s60, 9\n"
"s_mul_i32 s61, s60, 9\n"
"s_sub_u32 s61, s66, s61\n"
"s_cmp_ge_u32 s46, 4\n"
"s_cbranch_scc1 LR1H_%=\n"
"buffer_load_dwordx4 v250, s[24:27], s70 offen offset:2048 lds\n"
"buffer_load_dwordx4 v250, s[24:27], s71 offen offset:3072 lds\n"
"s_branch LR1E_%=\n"
"LR1H_%=:\n"
"s_add_u32 m0, s57, 49152\n"
"s_nop 0\n"
"buffer_load_dwordx4 v136, s[36:39], s58 offen lds\n"
"buffer_load_dwordx4 v137, s[36:39], s58 offen offset:1024 lds\n"
"buffer_load_dwordx4 v138, s[36:39], s58 offen offset:2048 lds\n"
"buffer_load_dwordx4 v139, s[36:39], s58 offen offset:3072 lds\n"
"s_add_u32 s58, s58, 128\n"
"s_cmp_eq_u32 s58, 1152\n"
"s_cselect_b32 s58, 0, s58\n"
"LR1E_%=:\n"
"s_lshl_b32 s61, s61, 3\n"
"s_sub_u32 s63, 31, s62\n"
"s_mul_i32 s63, s63, 19456\n"
"s_mul_i32 s64, s60, 9216\n"
"s_add_u32 s63, s63, s64\n"
"s_add_u32 s63, s63, 2415616\n"
"s_mul_i32 s64, s62, 9216\n"
"s_add_u32 s64, s64, 65536\n"
"s_cmp_eq_u32 s60, 0\n"
"s_cselect_b32 s53, s64, s63\n"
"s_cselect_b32 s55, 0x3eae1a93, 1.0\n"
"s_mov_b32 s63, 622592\n"
"s_cselect_b32 s63, 294912, s63\n"
"s_lshr_b32 s64, s49, 1\n"
"s_lshl_b32 s64, s64, 9\n"
"s_add_u32 s53, s53, s64\n"
"s_cmp_eq_u32 s60, 2\n"
"s_cselect_b32 s64, 3, 0\n"
"s_lshr_b32 s60, s35, 4\n"
"s_add_u32 s61, s61, s60\n"
"v_add3_u32 v130, v128, s61, 0\n"
"v_mul_u32_u24 v131, 57, v130\n"
"v_lshrrev_b32 v131, 9, v131\n"
"v_mul_u32_u24 v247, 9, v131\n"
"v_sub_u32 v130, v130, v247\n"
"v_mul_u32_u24 v131, s63, v131\n"
"v_lshl_add_u32 v131, v130, 10, v131\n"
"v_and_b32 v130, s64, v130\n"
"v_lshlrev_b32 v130, 6, v130\n"
"v_xor_b32 v130, v130, v129\n"
"v_add_u32 v247, v131, v130\n"
"v_add3_u32 v130, v128, s61, 2\n"
"v_mul_u32_u24 v131, 57, v130\n"
"v_lshrrev_b32 v131, 9, v131\n"
"v_mul_u32_u24 v248, 9, v131\n"
"v_sub_u32 v130, v130, v248\n"
"v_mul_u32_u24 v131, s63, v131\n"
"v_lshl_add_u32 v131, v130, 10, v131\n"
"v_and_b32 v130, s64, v130\n"
"v_lshlrev_b32 v130, 6, v130\n"
"v_xor_b32 v130, v130, v129\n"
"v_add_u32 v248, v131, v130\n"
"s_cmp_ge_u32 s48, 3\n"
"s_cselect_b32 s60, 0x40000000, 0\n"
"v_or_b32 v248, s60, v248\n"
"s_and_b32 s60, s49, 1\n"
"s_lshl_b32 s60, s60, 12\n"
"s_lshr_b32 s61, s49, 1\n"
"s_lshl_b32 s61, s61, 11\n"
"s_add_u32 s60, s60, s61\n"
"s_cmp_lt_u32 s46, 4\n"
"s_cselect_b32 s61, 122880, 0\n"
"s_add_u32 s60, s60, s61\n"
"v_add_u32 v249, s60, v255\n"
"s_mov_b32 s56, 0xbfb8aa3b\n"
"s_lshl_b32 s60, s62, 3\n"
"s_add_u32 s60, s60, s48\n"
"s_mul_i32 s41, s60, 36864\n"
"s_add_u32 s41, s41, 7405568\n"
"s_lshr_b32 s95, s46, 1\n"
"s_and_b32 s94, s46, 1\n"
"s_lshl_b32 s60, s48, 5\n"
"s_add_u32 s60, s60, 31\n"
"s_sub_u32 s60, s60, s47\n"
"s_mul_i32 s60, s60, 19456\n"
"s_add_u32 s60, s60, 2424832\n"
"s_add_u32 s20, s18, s60\n"
"s_addc_u32 s21, s19, 0\n"
"s_min_u32 s60, s47, 4\n"
"s_add_u32 s60, s60, 1\n"
"s_mul_i32 s22, s60, 19456\n"
"s_mov_b32 s23, 0x20000\n"
"v_lshrrev_b32 v128, 5, v140\n"
"v_and_b32 v129, 0x1f0, v255\n"
"s_lshl_b32 s60, s95, 11\n"
"s_lshl_b32 s61, s94, 9\n"
"s_add_u32 s62, s60, s61\n"
"v_lshl_add_u32 v130, v128, 10, v129\n"
"v_add_u32 v224, s62, v130\n"
"s_sub_u32 s63, 16384, s60\n"
"v_mad_u32_u24 v130, v128, s63, v129\n"
"s_add_u32 s62, s62, 2048\n"
"v_add_u32 v226, s62, v130\n"
"s_mul_i32 s63, s94, 20480\n"
"s_sub_u32 s63, 19968, s63\n"
"v_add_u32 v225, s63, v224\n"
"v_add_u32 v227, s63, v226\n"
"s_mul_i32 s60, s95, 9\n"
"s_lshr_b32 s60, s60, 1\n"
"v_bfe_u32 v129, v140, 4, 1\n"
"v_and_b32 v130, 3, v140\n"
"v_lshl_add_u32 v129, v129, 2, v130\n"
"v_cmp_lt_u32 vcc, 4, v129\n"
"v_add_u32 v129, s60, v129\n"
"v_lshrrev_b32 v130, 1, v129\n"
"s_lshr_b32 s60, s60, 1\n"
"s_add_u32 s60, s60, 3\n"
"v_mov_b32 v131, s60\n"
"v_cndmask_b32 v131, v130, v131, vcc\n"
"v_xor_b32 v131, v131, v128\n"
"v_and_b32 v131, 3, v131\n"
"v_mov_b32 v229, 9\n"
"v_cndmask_b32 v130, v130, v229, vcc\n"
"v_and_b32 v129, 1, v129\n"
"v_lshlrev_b32 v129, 3, v129\n"
"v_lshl_add_u32 v129, v130, 10, v129\n"
"v_lshl_add_u32 v129, v131, 6, v129\n"
"v_and_b32 v130, 12, v140\n"
"v_lshl_add_u32 v129, v130, 2, v129\n"
"s_add_u32 s61, s61, 9216\n"
"v_add_u32 v228, s61, v129\n"
"v_xor_b32 v229, 0x80, v228\n"
"v_add_u32 v230, s63, v228\n"
"v_add_u32 v231, s63, v229\n"
"v_and_b32 v129, 31, v140\n"
"v_lshlrev_b32 v130, 2, v128\n"
"v_sub_u32 v129, v129, v130\n"
"v_mov_b32 v130, 0xffff\n"
"v_cmp_le_i32 s[60:61], 0, v129\n"
"v_cmp_gt_i32 s[62:63], 1, v129\n"
"v_cmp_le_i32 s[64:65], 2, v129\n"
"v_cmp_gt_i32 s[66:67], 3, v129\n"
"v_cndmask_b32 v216, 0, v130, s[60:61]\n"
"v_cndmask_b32 v216, -1, v216, s[62:63]\n"
"v_cndmask_b32 v217, 0, v130, s[64:65]\n"
"v_cndmask_b32 v217, -1, v217, s[66:67]\n"
"v_cmp_le_i32 s[60:61], 8, v129\n"
"v_cmp_gt_i32 s[62:63], 9, v129\n"
"v_cmp_le_i32 s[64:65], 10, v129\n"
"v_cmp_gt_i32 s[66:67], 11, v129\n"
"v_cndmask_b32 v218, 0, v130, s[60:61]\n"
"v_cndmask_b32 v218, -1, v218, s[62:63]\n"
"v_cndmask_b32 v219, 0, v130, s[64:65]\n"
"v_cndmask_b32 v219, -1, v219, s[66:67]\n"
"v_cmp_le_i32 s[60:61], 16, v129\n"
"v_cmp_gt_i32 s[62:63], 17, v129\n"
"v_cmp_le_i32 s[64:65], 18, v129\n"
"v_cmp_gt_i32 s[66:67], 19, v129\n"
"v_cndmask_b32 v220, 0, v130, s[60:61]\n"
"v_cndmask_b32 v220, -1, v220, s[62:63]\n"
"v_cndmask_b32 v221, 0, v130, s[64:65]\n"
"v_cndmask_b32 v221, -1, v221, s[66:67]\n"
"v_cmp_le_i32 s[60:61], 24, v129\n"
"v_cmp_gt_i32 s[62:63], 25, v129\n"
"v_cmp_le_i32 s[64:65], 26, v129\n"
"v_cmp_gt_i32 s[66:67], 27, v129\n"
"v_cndmask_b32 v222, 0, v130, s[60:61]\n"
"v_cndmask_b32 v222, -1, v222, s[62:63]\n"
"v_cndmask_b32 v223, 0, v130, s[64:65]\n"
"v_cndmask_b32 v223, -1, v223, s[66:67]\n"
"s_cmp_gt_u32 s95, 0\n"
"s_cselect_b32 s88, 0, -1\n"
"s_not_b32 s91, s88\n"
"s_cmp_gt_u32 s95, 1\n"
"s_cselect_b32 s89, 0, -1\n"
"s_not_b32 s92, s89\n"
"s_cmp_gt_u32 s95, 2\n"
"s_cselect_b32 s90, 0, -1\n"
"s_not_b32 s93, s90\n"
"v_and_b32 v129, 15, v140\n"
"v_bfe_u32 v130, v140, 4, 1\n"
"s_lshl_b32 s60, s94, 1\n"
"v_add_u32 v130, s60, v130\n"
"v_lshl_add_u32 v131, v128, 4, v129\n"
"v_lshlrev_b32 v131, 4, v131\n"
"v_lshl_add_u32 v131, v130, 10, v131\n"
"s_lshr_b32 s60, s95, 1\n"
"s_and_b32 s61, s95, 1\n"
"s_lshl_b32 s62, s60, 15\n"
"s_lshl_b32 s63, s61, 9\n"
"s_add_u32 s62, s62, s63\n"
"s_add_u32 s62, s62, 28672\n"
"v_add_u32 v232, s62, v131\n"
"v_lshl_add_u32 v129, v128, 5, v129\n"
"v_lshlrev_b32 v129, 3, v129\n"
"v_lshl_add_u32 v129, v130, 9, v129\n"
"s_lshl_b32 s60, s60, 7\n"
"s_lshl_b32 s62, s61, 2\n"
"s_add_u32 s60, s60, s62\n"
"s_add_u32 s60, s60, 94208\n"
"v_add_u32 v233, s60, v129\n"
"v_bfe_u32 v128, v140, 2, 3\n"
"v_and_b32 v129, 6, v128\n"
"v_bfe_u32 v130, v140, 1, 1\n"
"s_lshl_b32 s61, s49, 1\n"
"v_or_b32 v130, s61, v130\n"
"v_xor_b32 v129, v129, v130\n"
"v_lshlrev_b32 v129, 4, v129\n"
"v_and_b32 v130, 1, v140\n"
"v_lshl_add_u32 v129, v130, 3, v129\n"
"v_lshl_add_u32 v129, v128, 7, v129\n"
"v_lshrrev_b32 v128, 5, v140\n"
"v_mul_u32_u24 v128, 7168, v128\n"
"v_add_u32 v234, v128, v129\n"
"v_lshrrev_b32 v130, 3, v140\n"
"v_and_b32 v131, 6, v130\n"
"v_and_b32 v129, 7, v140\n"
"v_xor_b32 v131, v131, v129\n"
"v_lshlrev_b32 v131, 4, v131\n"
"s_cmp_ge_u32 s46, 4\n"
"s_cbranch_scc1 LR2H_%=\n"
"s_add_u32 m0, s57, 102400\n"
"s_nop 0\n"
"buffer_load_dwordx4 v250, s[24:27], s72 offen lds\n"
"buffer_load_dwordx4 v250, s[24:27], s73 offen offset:1024 lds\n"
"s_branch LR2E_%=\n"
"LR2H_%=:\n"
"s_add_u32 m0, s57, 73728\n"
"s_nop 0\n"
"buffer_load_dwordx4 v136, s[36:39], s58 offen lds\n"
"buffer_load_dwordx4 v137, s[36:39], s58 offen offset:1024 lds\n"
"buffer_load_dwordx4 v138, s[36:39], s58 offen offset:2048 lds\n"
"buffer_load_dwordx4 v139, s[36:39], s58 offen offset:3072 lds\n"
"s_add_u32 s58, s58, 128\n"
"s_cmp_eq_u32 s58, 1152\n"
"s_cselect_b32 s58, 0, s58\n"
"LR2E_%=:\n"
"s_cmp_ge_u32 s46, 4\n"
"s_cbranch_scc1 LPH_%=\n"
"s_waitcnt vmcnt(4)\n"
"s_barrier\n"
"ds_read_b128 v[0:3], v186 offset:0\n"
"ds_read_b64_tr_b16 v[200:201], v253 offset:0\n"
"ds_read_b64_tr_b16 v[202:203], v253 offset:512\n"
"ds_read_b128 v[4:7], v186 offset:2048\n"
"ds_read_b64_tr_b16 v[204:205], v254 offset:0\n"
"ds_read_b64_tr_b16 v[206:207], v254 offset:512\n"
"ds_read_b128 v[8:11], v187 offset:0\n"
"ds_read_b64_tr_b16 v[208:209], v253 offset:1024\n"
"ds_read_b64_tr_b16 v[210:211], v253 offset:1536\n"
"ds_read_b128 v[12:15], v187 offset:2048\n"
"ds_read_b64_tr_b16 v[212:213], v254 offset:1024\n"
"ds_read_b64_tr_b16 v[214:215], v254 offset:1536\n"
"s_add_u32 m0, s57, 0\n"
"s_nop 0\n"
"buffer_load_dwordx4 v250, s[24:27], s74 offen lds\n"
"buffer_load_dwordx4 v250, s[24:27], s75 offen offset:1024 lds\n"
"s_waitcnt lgkmcnt(0)\n"
"s_waitcnt vmcnt(4)\n"
"s_barrier\n"
"ds_read_b128 v[16:19], v184 offset:0\n"
"ds_read_b64_tr_b16 v[144:145], v253 offset:2048\n"
"v_mfma_f32_16x16x32_bf16 v[168:171], v[200:203], v[0:3], 0\n"
"s_add_u32 m0, s57, 0\n"
"s_nop 0\n"
"buffer_load_dwordx4 v250, s[24:27], s76 offen offset:2048 lds\n"
"s_movk_i32 s60, 1152\n"
"ds_read_b64_tr_b16 v[146:147], v253 offset:2560\n"
"ds_read_b128 v[20:23], v184 offset:2048\n"
"v_mfma_f32_16x16x32_bf16 v[172:175], v[200:203], v[4:7], 0\n"
"buffer_load_dwordx4 v250, s[24:27], s77 offen offset:3072 lds\n"
"s_lshl_b32 s61, s49, 3\n"
"ds_read_b64_tr_b16 v[148:149], v254 offset:2048\n"
"ds_read_b64_tr_b16 v[150:151], v254 offset:2560\n"
"v_mfma_f32_16x16x32_bf16 v[176:179], v[204:207], v[0:3], 0\n"
"ds_read_b128 v[24:27], v185 offset:0\n"
"ds_read_b64_tr_b16 v[152:153], v253 offset:3072\n"
"v_mfma_f32_16x16x32_bf16 v[180:183], v[204:207], v[4:7], 0\n"
"v_add_u32 v129, s61, v130\n"
"ds_read_b64_tr_b16 v[154:155], v253 offset:3584\n"
"ds_read_b128 v[28:31], v185 offset:2048\n"
"v_mfma_f32_16x16x32_bf16 v[168:171], v[208:211], v[8:11], v[168:171]\n"
"v_mad_u32_u24 v235, v129, s60, v131\n"
"ds_read_b64_tr_b16 v[156:157], v254 offset:3072\n"
"ds_read_b64_tr_b16 v[158:159], v254 offset:3584\n"
"v_mfma_f32_16x16x32_bf16 v[172:175], v[208:211], v[12:15], v[172:175]\n"
"v_mfma_f32_16x16x32_bf16 v[176:179], v[212:215], v[8:11], v[176:179]\n"
"s_lshr_b32 s61, s49, 1\n"
"v_mfma_f32_16x16x32_bf16 v[180:183], v[212:215], v[12:15], v[180:183]\n"
"s_lshl_b32 s61, s61, 3\n"
"s_waitcnt lgkmcnt(0)\n"
"s_waitcnt vmcnt(4)\n"
"s_barrier\n"
"ds_read_b128 v[32:35], v184 offset:24576\n"
"ds_read_b64_tr_b16 v[200:201], v253 offset:4096\n"
"v_mfma_f32_16x16x32_bf16 v[168:171], v[144:147], v[16:19], v[168:171]\n"
"s_add_u32 m0, s57, 4096\n"
"s_nop 0\n"
"buffer_load_dwordx4 v250, s[24:27], s78 offen lds\n"
"ds_read_b64_tr_b16 v[202:203], v253 offset:4608\n"
"ds_read_b128 v[36:39], v184 offset:26624\n"
"v_mfma_f32_16x16x32_bf16 v[172:175], v[144:147], v[20:23], v[172:175]\n"
"buffer_load_dwordx4 v250, s[24:27], s79 offen offset:1024 lds\n"
"s_and_b32 s62, s49, 1\n"
"ds_read_b64_tr_b16 v[204:205], v254 offset:4096\n"
"ds_read_b64_tr_b16 v[206:207], v254 offset:4608\n"
"v_mfma_f32_16x16x32_bf16 v[176:179], v[148:151], v[16:19], v[176:179]\n"
"s_lshl_b32 s62, s62, 4\n"
"ds_read_b128 v[40:43], v185 offset:24576\n"
"ds_read_b64_tr_b16 v[208:209], v253 offset:5120\n"
"v_mfma_f32_16x16x32_bf16 v[180:183], v[148:151], v[20:23], v[180:183]\n"
"ds_read_b64_tr_b16 v[210:211], v253 offset:5632\n"
"ds_read_b128 v[44:47], v185 offset:26624\n"
"v_mfma_f32_16x16x32_bf16 v[168:171], v[152:155], v[24:27], v[168:171]\n"
"s_add_u32 s61, s61, s62\n"
"ds_read_b64_tr_b16 v[212:213], v254 offset:5120\n"
"ds_read_b64_tr_b16 v[214:215], v254 offset:5632\n"
"v_mfma_f32_16x16x32_bf16 v[172:175], v[152:155], v[28:31], v[172:175]\n"
"v_add_u32 v130, s61, v130\n"
"v_mfma_f32_16x16x32_bf16 v[176:179], v[156:159], v[24:27], v[176:179]\n"
"v_mfma_f32_16x16x32_bf16 v[180:183], v[156:159], v[28:31], v[180:183]\n"
"s_cmp_eq_u32 s49, 1\n"
"s_cselect_b32 vcc_lo, 0xffff, 0\n"
"s_waitcnt lgkmcnt(0)\n"
"s_waitcnt vmcnt(4)\n"
"s_barrier\n"
"ds_read_b128 v[48:51], v186 offset:16384\n"
"ds_read_b64_tr_b16 v[144:145], v252 offset:0\n"
"v_mfma_f32_16x16x32_bf16 v[168:171], v[200:203], v[32:35], v[168:171]\n"
"s_add_u32 m0, s57, 24576\n"
"s_nop 0\n"
"buffer_load_dwordx4 v250, s[24:27], s80 offen lds\n"
"ds_read_b64_tr_b16 v[146:147], v252 offset:512\n"
"ds_read_b128 v[52:55], v186 offset:18432\n"
"v_mfma_f32_16x16x32_bf16 v[172:175], v[200:203], v[36:39], v[172:175]\n"
"buffer_load_dwordx4 v250, s[24:27], s81 offen offset:1024 lds\n"
"ds_read_b64_tr_b16 v[148:149], v188 offset:0\n"
"ds_read_b64_tr_b16 v[150:151], v188 offset:512\n"
"v_mfma_f32_16x16x32_bf16 v[176:179], v[204:207], v[32:35], v[176:179]\n"
"s_mov_b32 vcc_hi, 0\n"
"ds_read_b128 v[56:59], v187 offset:16384\n"
"ds_read_b64_tr_b16 v[152:153], v252 offset:1024\n"
"v_mfma_f32_16x16x32_bf16 v[180:183], v[204:207], v[36:39], v[180:183]\n"
"ds_read_b64_tr_b16 v[154:155], v252 offset:1536\n"
"ds_read_b128 v[60:63], v187 offset:18432\n"
"v_mfma_f32_16x16x32_bf16 v[168:171], v[208:211], v[40:43], v[168:171]\n"
"v_add_u32 v129, 18, v130\n"
"ds_read_b64_tr_b16 v[156:157], v188 offset:1024\n"
"ds_read_b64_tr_b16 v[158:159], v188 offset:1536\n"
"v_mfma_f32_16x16x32_bf16 v[172:175], v[208:211], v[44:47], v[172:175]\n"
"v_cndmask_b32 v130, v130, v129, vcc\n"
"v_mfma_f32_16x16x32_bf16 v[176:179], v[212:215], v[40:43], v[176:179]\n"
"v_mfma_f32_16x16x32_bf16 v[180:183], v[212:215], v[44:47], v[180:183]\n"
"v_mad_u32_u24 v244, v130, s60, v131\n"
"s_waitcnt lgkmcnt(0)\n"
"s_waitcnt vmcnt(4)\n"
"s_barrier\n"
"ds_read_b128 v[64:67], v186 offset:0\n"
"ds_read_b64_tr_b16 v[200:201], v252 offset:2048\n"
"v_mfma_f32_16x16x32_bf16 v[168:171], v[144:147], v[48:51], v[168:171]\n"
"s_add_u32 m0, s57, 24576\n"
"s_nop 0\n"
"buffer_load_dwordx4 v250, s[24:27], s82 offen offset:2048 lds\n"
"s_mul_i32 s62, s51, 0x4c957f2d\n"
"ds_read_b64_tr_b16 v[202:203], v252 offset:2560\n"
"ds_read_b128 v[68:71], v186 offset:2048\n"
"v_mfma_f32_16x16x32_bf16 v[172:175], v[144:147], v[52:55], v[172:175]\n"
"buffer_load_dwordx4 v250, s[24:27], s83 offen offset:3072 lds\n"
"ds_read_b64_tr_b16 v[204:205], v188 offset:2048\n"
"ds_read_b64_tr_b16 v[206:207], v188 offset:2560\n"
"v_mfma_f32_16x16x32_bf16 v[176:179], v[148:151], v[48:51], v[176:179]\n"
"s_mul_hi_u32 s51, s50, 0x4c957f2d\n"
"ds_read_b128 v[72:75], v187 offset:0\n"
"ds_read_b64_tr_b16 v[208:209], v252 offset:3072\n"
"v_mfma_f32_16x16x32_bf16 v[180:183], v[148:151], v[52:55], v[180:183]\n"
"s_add_u32 s51, s51, s62\n"
"ds_read_b64_tr_b16 v[210:211], v252 offset:3584\n"
"ds_read_b128 v[76:79], v187 offset:2048\n"
"v_mfma_f32_16x16x32_bf16 v[168:171], v[152:155], v[56:59], v[168:171]\n"
"ds_read_b64_tr_b16 v[212:213], v188 offset:3072\n"
"ds_read_b64_tr_b16 v[214:215], v188 offset:3584\n"
"v_mfma_f32_16x16x32_bf16 v[172:175], v[152:155], v[60:63], v[172:175]\n"
"s_mul_i32 s62, s50, 0x5851f42d\n"
"v_mfma_f32_16x16x32_bf16 v[176:179], v[156:159], v[56:59], v[176:179]\n"
"s_add_u32 s51, s51, s62\n"
"v_mfma_f32_16x16x32_bf16 v[180:183], v[156:159], v[60:63], v[180:183]\n"
"s_waitcnt lgkmcnt(0)\n"
"s_waitcnt vmcnt(4)\n"
"s_barrier\n"
"ds_read_b128 v[80:83], v184 offset:0\n"
"ds_read_b64_tr_b16 v[144:145], v252 offset:4096\n"
"v_mfma_f32_16x16x32_bf16 v[168:171], v[200:203], v[64:67], v[168:171]\n"
"s_add_u32 m0, s57, 28672\n"
"s_nop 0\n"
"buffer_load_dwordx4 v250, s[24:27], s84 offen lds\n"
"s_mul_i32 s50, s50, 0x4c957f2d\n"
"ds_read_b64_tr_b16 v[146:147], v252 offset:4608\n"
"ds_read_b128 v[84:87], v184 offset:2048\n"
"v_mfma_f32_16x16x32_bf16 v[172:175], v[200:203], v[68:71], v[172:175]\n"
"buffer_load_dwordx4 v250, s[24:27], s85 offen offset:1024 lds\n"
"ds_read_b64_tr_b16 v[148:149], v188 offset:4096\n"
"ds_read_b64_tr_b16 v[150:151], v188 offset:4608\n"
"v_mfma_f32_16x16x32_bf16 v[176:179], v[204:207], v[64:67], v[176:179]\n"
"s_add_u32 s50, s50, 0xf767814f\n"
"s_addc_u32 s51, s51, 0x14057b7e\n"
"ds_read_b128 v[88:91], v185 offset:0\n"
"ds_read_b64_tr_b16 v[152:153], v252 offset:5120\n"
"v_mfma_f32_16x16x32_bf16 v[180:183], v[204:207], v[68:71], v[180:183]\n"
"ds_read_b64_tr_b16 v[154:155], v252 offset:5632\n"
"ds_read_b128 v[92:95], v185 offset:2048\n"
"v_mfma_f32_16x16x32_bf16 v[168:171], v[208:211], v[72:75], v[168:171]\n"
"ds_read_b64_tr_b16 v[156:157], v188 offset:5120\n"
"ds_read_b64_tr_b16 v[158:159], v188 offset:5632\n"
"v_mfma_f32_16x16x32_bf16 v[172:175], v[208:211], v[76:79], v[172:175]\n"
"s_xor_b32 s60, s47, 1\n"
"v_mfma_f32_16x16x32_bf16 v[176:179], v[212:215], v[72:75], v[176:179]\n"
"s_lshl_b32 s60, s60, 3\n"
"v_mfma_f32_16x16x32_bf16 v[180:183], v[212:215], v[76:79], v[180:183]\n"
"s_waitcnt lgkmcnt(0)\n"
"s_waitcnt vmcnt(4)\n"
"s_barrier\n"
"ds_read_b128 v[96:99], v184 offset:24576\n"
"ds_read_b64_tr_b16 v[200:201], v252 offset:24576\n"
"v_mfma_f32_16x16x32_bf16 v[168:171], v[144:147], v[80:83], v[168:171]\n"
"v_add_u32 v250, s58, v250\n"
"s_add_u32 m0, s57, 49152\n"
"s_nop 0\n"
"buffer_load_dwordx4 v250, s[24:27], s68 offen lds\n"
"s_add_u32 s60, s60, s48\n"
"ds_read_b64_tr_b16 v[202:203], v252 offset:25088\n"
"ds_read_b128 v[100:103], v184 offset:26624\n"
"v_mfma_f32_16x16x32_bf16 v[172:175], v[144:147], v[84:87], v[172:175]\n"
"buffer_load_dwordx4 v250, s[24:27], s69 offen offset:1024 lds\n"
"v_readlane_b32 s68, v251, 0\n"
"s_lshl_b32 s60, s60, 2\n"
"ds_read_b64_tr_b16 v[204:205], v188 offset:24576\n"
"ds_read_b64_tr_b16 v[206:207], v188 offset:25088\n"
"v_mfma_f32_16x16x32_bf16 v[176:179], v[148:151], v[80:83], v[176:179]\n"
"buffer_load_dwordx4 v250, s[24:27], s70 offen offset:2048 lds\n"
"v_readlane_b32 s69, v251, 1\n"
"ds_read_b128 v[104:107], v185 offset:24576\n"
"ds_read_b64_tr_b16 v[208:209], v252 offset:25600\n"
"v_mfma_f32_16x16x32_bf16 v[180:183], v[148:151], v[84:87], v[180:183]\n"
"buffer_load_dwordx4 v250, s[24:27], s71 offen offset:3072 lds\n"
"v_readlane_b32 s70, v251, 2\n"
"s_add_u32 s60, s60, s49\n"
"ds_read_b64_tr_b16 v[210:211], v252 offset:26112\n"
"ds_read_b128 v[108:111], v185 offset:26624\n"
"v_mfma_f32_16x16x32_bf16 v[168:171], v[152:155], v[88:91], v[168:171]\n"
"s_add_u32 m0, s57, 53248\n"
"s_nop 0\n"
"buffer_load_dwordx4 v250, s[24:27], s72 offen lds\n"
"v_readlane_b32 s71, v251, 3\n"
"s_lshl_b32 s60, s60, 4\n"
"ds_read_b64_tr_b16 v[212:213], v188 offset:25600\n"
"ds_read_b64_tr_b16 v[214:215], v188 offset:26112\n"
"v_mfma_f32_16x16x32_bf16 v[172:175], v[152:155], v[92:95], v[172:175]\n"
"buffer_load_dwordx4 v250, s[24:27], s73 offen offset:1024 lds\n"
"v_readlane_b32 s72, v251, 4\n"
"v_mfma_f32_16x16x32_bf16 v[176:179], v[156:159], v[88:91], v[176:179]\n"
"s_add_u32 s100, s60, 49152\n"
"v_mfma_f32_16x16x32_bf16 v[180:183], v[156:159], v[92:95], v[180:183]\n"
"s_waitcnt lgkmcnt(0)\n"
"s_waitcnt vmcnt(8)\n"
"s_barrier\n"
"ds_read_b128 v[112:115], v186 offset:16384\n"
"ds_read_b64_tr_b16 v[144:145], v252 offset:26624\n"
"v_mfma_f32_16x16x32_bf16 v[168:171], v[200:203], v[96:99], v[168:171]\n"
"s_add_u32 m0, s57, 73728\n"
"s_nop 0\n"
"buffer_load_dwordx4 v250, s[24:27], s74 offen lds\n"
"v_readlane_b32 s73, v251, 5\n"
"ds_read_b64_tr_b16 v[146:147], v252 offset:27136\n"
"ds_read_b128 v[116:119], v186 offset:18432\n"
"v_mfma_f32_16x16x32_bf16 v[172:175], v[200:203], v[100:103], v[172:175]\n"
"buffer_load_dwordx4 v250, s[24:27], s75 offen offset:1024 lds\n"
"v_readlane_b32 s74, v251, 6\n"
"ds_read_b64_tr_b16 v[148:149], v188 offset:26624\n"
"ds_read_b64_tr_b16 v[150:151], v188 offset:27136\n"
"v_mfma_f32_16x16x32_bf16 v[176:179], v[204:207], v[96:99], v[176:179]\n"
"buffer_load_dwordx4 v250, s[24:27], s76 offen offset:2048 lds\n"
"v_readlane_b32 s75, v251, 7\n"
"ds_read_b128 v[120:123], v187 offset:16384\n"
"ds_read_b64_tr_b16 v[152:153], v252 offset:27648\n"
"v_mfma_f32_16x16x32_bf16 v[180:183], v[204:207], v[100:103], v[180:183]\n"
"buffer_load_dwordx4 v250, s[24:27], s77 offen offset:3072 lds\n"
"v_readlane_b32 s76, v251, 8\n"
"ds_read_b64_tr_b16 v[154:155], v252 offset:28160\n"
"ds_read_b128 v[124:127], v187 offset:18432\n"
"v_mfma_f32_16x16x32_bf16 v[168:171], v[208:211], v[104:107], v[168:171]\n"
"s_add_u32 m0, s57, 77824\n"
"s_nop 0\n"
"buffer_load_dwordx4 v250, s[24:27], s78 offen lds\n"
"v_readlane_b32 s77, v251, 9\n"
"ds_read_b64_tr_b16 v[156:157], v188 offset:27648\n"
"ds_read_b64_tr_b16 v[158:159], v188 offset:28160\n"
"v_mfma_f32_16x16x32_bf16 v[172:175], v[208:211], v[108:111], v[172:175]\n"
"buffer_load_dwordx4 v250, s[24:27], s79 offen offset:1024 lds\n"
"v_readlane_b32 s78, v251, 10\n"
"v_mfma_f32_16x16x32_bf16 v[176:179], v[212:215], v[104:107], v[176:179]\n"
"v_mfma_f32_16x16x32_bf16 v[180:183], v[212:215], v[108:111], v[180:183]\n"
"s_waitcnt lgkmcnt(0)\n"
"s_waitcnt vmcnt(12)\n"
"s_barrier\n"
"ds_read_b128 v[128:131], v186 offset:0\n"
"ds_read_b64_tr_b16 v[200:201], v252 offset:28672\n"
"v_mfma_f32_16x16x32_bf16 v[168:171], v[144:147], v[112:115], v[168:171]\n"
"s_add_u32 m0, s57, 98304\n"
"s_nop 0\n"
"buffer_load_dwordx4 v250, s[24:27], s80 offen lds\n"
"v_readlane_b32 s79, v251, 11\n"
"ds_read_b64_tr_b16 v[202:203], v252 offset:29184\n"
"ds_read_b128 v[132:135], v186 offset:2048\n"
"v_mfma_f32_16x16x32_bf16 v[172:175], v[144:147], v[116:119], v[172:175]\n"
"buffer_load_dwordx4 v250, s[24:27], s81 offen offset:1024 lds\n"
"v_readlane_b32 s80, v251, 12\n"
"ds_read_b64_tr_b16 v[204:205], v188 offset:28672\n"
"ds_read_b64_tr_b16 v[206:207], v188 offset:29184\n"
"v_mfma_f32_16x16x32_bf16 v[176:179], v[148:151], v[112:115], v[176:179]\n"
"buffer_load_dwordx4 v250, s[24:27], s82 offen offset:2048 lds\n"
"v_readlane_b32 s81, v251, 13\n"
"ds_read_b128 v[136:139], v187 offset:0\n"
"ds_read_b64_tr_b16 v[208:209], v252 offset:29696\n"
"v_mfma_f32_16x16x32_bf16 v[180:183], v[148:151], v[116:119], v[180:183]\n"
"buffer_load_dwordx4 v250, s[24:27], s83 offen offset:3072 lds\n"
"v_readlane_b32 s82, v251, 14\n"
"ds_read_b64_tr_b16 v[210:211], v252 offset:30208\n"
"ds_read_b128 v[140:143], v187 offset:2048\n"
"v_mfma_f32_16x16x32_bf16 v[168:171], v[152:155], v[120:123], v[168:171]\n"
"s_add_u32 m0, s57, 102400\n"
"s_nop 0\n"
"buffer_load_dwordx4 v250, s[24:27], s84 offen lds\n"
"v_readlane_b32 s83, v251, 15\n"
"ds_read_b64_tr_b16 v[212:213], v188 offset:29696\n"
"ds_read_b64_tr_b16 v[214:215], v188 offset:30208\n"
"v_mfma_f32_16x16x32_bf16 v[172:175], v[152:155], v[124:127], v[172:175]\n"
"buffer_load_dwordx4 v250, s[24:27], s85 offen offset:1024 lds\n"
"v_readlane_b32 s84, v251, 16\n"
"v_mfma_f32_16x16x32_bf16 v[176:179], v[156:159], v[120:123], v[176:179]\n"
"v_mfma_f32_16x16x32_bf16 v[180:183], v[156:159], v[124:127], v[180:183]\n"
"v_xor_b32 v252, s35, v252\n"
"v_add_u32 v253, 49152, v252\n"
"v_xor_b32 v254, 32, v253\n"
"s_mov_b32 s42, 73728\n"
"s_mov_b32 s43, 24576\n"
"s_mov_b32 s44, 49152\n"
"s_waitcnt lgkmcnt(0)\n"
"s_waitcnt vmcnt(12)\n"
"s_barrier\n"
"v_readlane_b32 s85, v251, 17\n"
"v_mov_b32 v250, v189\n"
"s_sub_u32 s24, s10, 3072\n"
"s_subb_u32 s25, s11, 0\n"
"s_mov_b32 s26, 5311488\n"
"s_add_u32 m0, s57, 0\n"
"s_nop 0\n"
"buffer_load_dwordx4 v250, s[24:27], s68 offen lds\n"
"ds_read_b64_tr_b16 v[144:145], v253 offset:0\n"
"ds_read_b64_tr_b16 v[146:147], v253 offset:512\n"
"v_mfma_f32_16x16x32_bf16 v[168:171], v[200:203], v[128:131], v[168:171]\n"
"buffer_load_dwordx4 v250, s[24:27], s69 offen offset:1024 lds\n"
"ds_read_b64_tr_b16 v[148:149], v254 offset:0\n"
"ds_read_b64_tr_b16 v[150:151], v254 offset:512\n"
"v_mfma_f32_16x16x32_bf16 v[172:175], v[200:203], v[132:135], v[172:175]\n"
"buffer_load_dwordx4 v250, s[24:27], s70 offen offset:2048 lds\n"
"ds_read_b64_tr_b16 v[152:153], v253 offset:1024\n"
"ds_read_b64_tr_b16 v[154:155], v253 offset:1536\n"
"v_mfma_f32_16x16x32_bf16 v[176:179], v[204:207], v[128:131], v[176:179]\n"
"buffer_load_dwordx4 v250, s[24:27], s71 offen offset:3072 lds\n"
"ds_read_b64_tr_b16 v[156:157], v254 offset:1024\n"
"ds_read_b64_tr_b16 v[158:159], v254 offset:1536\n"
"v_mfma_f32_16x16x32_bf16 v[180:183], v[204:207], v[132:135], v[180:183]\n"
"s_add_u32 m0, s57, 4096\n"
"s_nop 0\n"
"buffer_load_dwordx4 v250, s[24:27], s72 offen lds\n"
"ds_read_b64_tr_b16 v[160:161], v253 offset:2048\n"
"ds_read_b64_tr_b16 v[162:163], v253 offset:2560\n"
"v_mfma_f32_16x16x32_bf16 v[184:187], v[208:211], v[136:139], v[168:171]\n"
"buffer_load_dwordx4 v250, s[24:27], s73 offen offset:1024 lds\n"
"ds_read_b64_tr_b16 v[164:165], v254 offset:2048\n"
"ds_read_b64_tr_b16 v[166:167], v254 offset:2560\n"
"v_mfma_f32_16x16x32_bf16 v[188:191], v[208:211], v[140:143], v[172:175]\n"
"s_add_u32 m0, s57, 24576\n"
"s_nop 0\n"
"buffer_load_dwordx4 v250, s[24:27], s74 offen lds\n"
"v_mfma_f32_16x16x32_bf16 v[192:195], v[212:215], v[136:139], v[176:179]\n"
"v_mfma_f32_16x16x32_bf16 v[196:199], v[212:215], v[140:143], v[180:183]\n"
"s_mov_b32 s45, 2\n"
"s_cmp_ge_u32 s48, 3\n"
"s_cbranch_scc1 LHBD_%=\n"
"LQD_%=:\n"
"s_waitcnt lgkmcnt(8)\n"
"v_mfma_f32_16x16x32_bf16 v[168:171], v[144:147], v[0:3], 0\n"
"v_mfma_f32_16x16x32_bf16 v[172:175], v[144:147], v[4:7], 0\n"
"ds_read_b64_tr_b16 v[144:145], v253 offset:3072\n"
"v_mfma_f32_16x16x32_bf16 v[176:179], v[148:151], v[0:3], 0\n"
"ds_read_b64_tr_b16 v[146:147], v253 offset:3584\n"
"v_mfma_f32_16x16x32_bf16 v[180:183], v[148:151], v[4:7], 0\n"
"ds_read_b64_tr_b16 v[148:149], v254 offset:3072\n"
"buffer_load_dwordx4 v250, s[24:27], s75 offen offset:1024 lds\n"
"s_waitcnt lgkmcnt(7)\n"
"v_mfma_f32_16x16x32_bf16 v[168:171], v[152:155], v[8:11], v[168:171]\n"
"ds_read_b64_tr_b16 v[150:151], v254 offset:3584\n"
"v_mfma_f32_16x16x32_bf16 v[172:175], v[152:155], v[12:15], v[172:175]\n"
"ds_read_b64_tr_b16 v[152:153], v253 offset:4096\n"
"v_mfma_f32_16x16x32_bf16 v[176:179], v[156:159], v[8:11], v[176:179]\n"
"ds_read_b64_tr_b16 v[154:155], v253 offset:4608\n"
"v_mfma_f32_16x16x32_bf16 v[180:183], v[156:159], v[12:15], v[180:183]\n"
"ds_read_b64_tr_b16 v[156:157], v254 offset:4096\n"
"buffer_load_dwordx4 v250, s[24:27], s76 offen offset:2048 lds\n"
"v_mul_f32 v184, s54, v184\n"
"v_mul_f32 v185, s54, v185\n"
"v_mul_f32 v186, s54, v186\n"
"v_mul_f32 v187, s54, v187\n"
"s_waitcnt lgkmcnt(7)\n"
"v_mfma_f32_16x16x32_bf16 v[168:171], v[160:163], v[16:19], v[168:171]\n"
"ds_read_b64_tr_b16 v[158:159], v254 offset:4608\n"
"v_mfma_f32_16x16x32_bf16 v[172:175], v[160:163], v[20:23], v[172:175]\n"
"ds_read_b64_tr_b16 v[160:161], v253 offset:5120\n"
"v_mfma_f32_16x16x32_bf16 v[176:179], v[164:167], v[16:19], v[176:179]\n"
"ds_read_b64_tr_b16 v[162:163], v253 offset:5632\n"
"v_mfma_f32_16x16x32_bf16 v[180:183], v[164:167], v[20:23], v[180:183]\n"
"ds_read_b64_tr_b16 v[164:165], v254 offset:5120\n"
"ds_read_b64_tr_b16 v[166:167], v254 offset:5632\n"
"buffer_load_dwordx4 v250, s[24:27], s77 offen offset:3072 lds\n"
"v_add_u32 v253, s42, v252\n"
"v_xor_b32 v254, 32, v253\n"
"v_mul_f32 v188, s54, v188\n"
"v_mul_f32 v189, s54, v189\n"
"v_mul_f32 v190, s54, v190\n"
"v_mul_f32 v191, s54, v191\n"
"s_waitcnt lgkmcnt(8)\n"
"v_mfma_f32_16x16x32_bf16 v[168:171], v[144:147], v[24:27], v[168:171]\n"
"v_mfma_f32_16x16x32_bf16 v[172:175], v[144:147], v[28:31], v[172:175]\n"
"s_add_u32 m0, m0, 4096\n"
"s_waitcnt vmcnt(16)\n"
"s_barrier\n"
"ds_read_b64_tr_b16 v[144:145], v253 offset:0\n"
"v_mfma_f32_16x16x32_bf16 v[176:179], v[148:151], v[24:27], v[176:179]\n"
"ds_read_b64_tr_b16 v[146:147], v253 offset:512\n"
"v_mfma_f32_16x16x32_bf16 v[180:183], v[148:151], v[28:31], v[180:183]\n"
"ds_read_b64_tr_b16 v[148:149], v254 offset:0\n"
"buffer_load_dwordx4 v250, s[24:27], s78 offen lds\n"
"v_cvt_pk_bf16_f32 v208, v184, v185\n"
"v_cvt_pk_bf16_f32 v209, v186, v187\n"
"v_cvt_pk_bf16_f32 v210, v188, v189\n"
"v_cvt_pk_bf16_f32 v211, v190, v191\n"
"s_waitcnt lgkmcnt(7)\n"
"v_mfma_f32_16x16x32_bf16 v[168:171], v[152:155], v[32:35], v[168:171]\n"
"ds_read_b64_tr_b16 v[150:151], v254 offset:512\n"
"s_add_u32 s43, s42, 24576\n"
"v_mfma_f32_16x16x32_bf16 v[172:175], v[152:155], v[36:39], v[172:175]\n"
"ds_read_b64_tr_b16 v[152:153], v253 offset:1024\n"
"s_cmp_eq_u32 s43, 122880\n"
"v_mfma_f32_16x16x32_bf16 v[176:179], v[156:159], v[32:35], v[176:179]\n"
"ds_read_b64_tr_b16 v[154:155], v253 offset:1536\n"
"s_cselect_b32 s43, 0, s43\n"
"v_mfma_f32_16x16x32_bf16 v[180:183], v[156:159], v[36:39], v[180:183]\n"
"ds_read_b64_tr_b16 v[156:157], v254 offset:1024\n"
"buffer_load_dwordx4 v250, s[24:27], s79 offen offset:1024 lds\n"
"v_permlane16_swap_b32 v208, v210\n"
"v_permlane16_swap_b32 v209, v211\n"
"buffer_store_dwordx4 v[208:211], v245, s[28:31], s52 offen sc1\n"
"s_waitcnt lgkmcnt(7)\n"
"v_mfma_f32_16x16x32_bf16 v[168:171], v[160:163], v[40:43], v[168:171]\n"
"ds_read_b64_tr_b16 v[158:159], v254 offset:1536\n"
"s_add_u32 m0, s44, s57\n"
"v_mfma_f32_16x16x32_bf16 v[172:175], v[160:163], v[44:47], v[172:175]\n"
"ds_read_b64_tr_b16 v[160:161], v253 offset:2048\n"
"v_mfma_f32_16x16x32_bf16 v[176:179], v[164:167], v[40:43], v[176:179]\n"
"ds_read_b64_tr_b16 v[162:163], v253 offset:2560\n"
"v_mfma_f32_16x16x32_bf16 v[180:183], v[164:167], v[44:47], v[180:183]\n"
"ds_read_b64_tr_b16 v[164:165], v254 offset:2048\n"
"ds_read_b64_tr_b16 v[166:167], v254 offset:2560\n"
"buffer_load_dwordx4 v250, s[24:27], s80 offen lds\n"
"v_mul_f32 v192, s54, v192\n"
"v_mul_f32 v193, s54, v193\n"
"v_mul_f32 v194, s54, v194\n"
"v_mul_f32 v195, s54, v195\n"
"s_waitcnt lgkmcnt(8)\n"
"v_mfma_f32_16x16x32_bf16 v[168:171], v[144:147], v[48:51], v[168:171]\n"
"v_mfma_f32_16x16x32_bf16 v[172:175], v[144:147], v[52:55], v[172:175]\n"
"ds_read_b64_tr_b16 v[144:145], v253 offset:3072\n"
"v_mfma_f32_16x16x32_bf16 v[176:179], v[148:151], v[48:51], v[176:179]\n"
"ds_read_b64_tr_b16 v[146:147], v253 offset:3584\n"
"v_mfma_f32_16x16x32_bf16 v[180:183], v[148:151], v[52:55], v[180:183]\n"
"ds_read_b64_tr_b16 v[148:149], v254 offset:3072\n"
"buffer_load_dwordx4 v250, s[24:27], s81 offen offset:1024 lds\n"
"v_mul_f32 v196, s54, v196\n"
"v_mul_f32 v197, s54, v197\n"
"v_mul_f32 v198, s54, v198\n"
"v_mul_f32 v199, s54, v199\n"
"s_waitcnt lgkmcnt(7)\n"
"v_mfma_f32_16x16x32_bf16 v[168:171], v[152:155], v[56:59], v[168:171]\n"
"ds_read_b64_tr_b16 v[150:151], v254 offset:3584\n"
"v_mfma_f32_16x16x32_bf16 v[172:175], v[152:155], v[60:63], v[172:175]\n"
"ds_read_b64_tr_b16 v[152:153], v253 offset:4096\n"
"v_mfma_f32_16x16x32_bf16 v[176:179], v[156:159], v[56:59], v[176:179]\n"
"ds_read_b64_tr_b16 v[154:155], v253 offset:4608\n"
"v_mfma_f32_16x16x32_bf16 v[180:183], v[156:159], v[60:63], v[180:183]\n"
"ds_read_b64_tr_b16 v[156:157], v254 offset:4096\n"
"buffer_load_dwordx4 v250, s[24:27], s82 offen offset:2048 lds\n"
"v_cvt_pk_bf16_f32 v212, v192, v193\n"
"v_cvt_pk_bf16_f32 v213, v194, v195\n"
"v_cvt_pk_bf16_f32 v214, v196, v197\n"
"v_cvt_pk_bf16_f32 v215, v198, v199\n"
"s_waitcnt lgkmcnt(7)\n"
"v_mfma_f32_16x16x32_bf16 v[168:171], v[160:163], v[64:67], v[168:171]\n"
"ds_read_b64_tr_b16 v[158:159], v254 offset:4608\n"
"v_mfma_f32_16x16x32_bf16 v[172:175], v[160:163], v[68:71], v[172:175]\n"
"ds_read_b64_tr_b16 v[160:161], v253 offset:5120\n"
"v_mfma_f32_16x16x32_bf16 v[176:179], v[164:167], v[64:67], v[176:179]\n"
"ds_read_b64_tr_b16 v[162:163], v253 offset:5632\n"
"v_mfma_f32_16x16x32_bf16 v[180:183], v[164:167], v[68:71], v[180:183]\n"
"ds_read_b64_tr_b16 v[164:165], v254 offset:5120\n"
"ds_read_b64_tr_b16 v[166:167], v254 offset:5632\n"
"buffer_load_dwordx4 v250, s[24:27], s83 offen offset:3072 lds\n"
"v_add_u32 v253, s43, v252\n"
"v_xor_b32 v254, 32, v253\n"
"v_permlane16_swap_b32 v212, v214\n"
"v_permlane16_swap_b32 v213, v215\n"
"buffer_store_dwordx4 v[212:215], v246, s[28:31], s52 offen sc1\n"
"s_waitcnt lgkmcnt(8)\n"
"v_mfma_f32_16x16x32_bf16 v[168:171], v[144:147], v[72:75], v[168:171]\n"
"v_mfma_f32_16x16x32_bf16 v[172:175], v[144:147], v[76:79], v[172:175]\n"
"s_add_u32 m0, m0, 4096\n"
"s_waitcnt vmcnt(18)\n"
"s_barrier\n"
"ds_read_b64_tr_b16 v[144:145], v253 offset:0\n"
"v_mfma_f32_16x16x32_bf16 v[176:179], v[148:151], v[72:75], v[176:179]\n"
"ds_read_b64_tr_b16 v[146:147], v253 offset:512\n"
"v_mfma_f32_16x16x32_bf16 v[180:183], v[148:151], v[76:79], v[180:183]\n"
"ds_read_b64_tr_b16 v[148:149], v254 offset:0\n"
"buffer_load_dwordx4 v250, s[24:27], s84 offen lds\n"
"v_mov_b32 v245, v247\n"
"v_mov_b32 v246, v248\n"
"s_mov_b32 s52, s53\n"
"s_mov_b32 s54, s55\n"
"s_waitcnt lgkmcnt(7)\n"
"v_mfma_f32_16x16x32_bf16 v[168:171], v[152:155], v[80:83], v[168:171]\n"
"ds_read_b64_tr_b16 v[150:151], v254 offset:512\n"
"s_add_u32 s44, s43, 24576\n"
"v_mfma_f32_16x16x32_bf16 v[172:175], v[152:155], v[84:87], v[172:175]\n"
"ds_read_b64_tr_b16 v[152:153], v253 offset:1024\n"
"s_cmp_eq_u32 s44, 122880\n"
"v_mfma_f32_16x16x32_bf16 v[176:179], v[156:159], v[80:83], v[176:179]\n"
"ds_read_b64_tr_b16 v[154:155], v253 offset:1536\n"
"s_cselect_b32 s44, 0, s44\n"
"v_mfma_f32_16x16x32_bf16 v[180:183], v[156:159], v[84:87], v[180:183]\n"
"ds_read_b64_tr_b16 v[156:157], v254 offset:1024\n"
"buffer_load_dwordx4 v250, s[24:27], s85 offen offset:1024 lds\n"
"v_add_u32 v250, s40, v250\n"
"s_waitcnt lgkmcnt(7)\n"
"v_mfma_f32_16x16x32_bf16 v[168:171], v[160:163], v[88:91], v[168:171]\n"
"ds_read_b64_tr_b16 v[158:159], v254 offset:1536\n"
"s_add_u32 m0, s42, s57\n"
"v_mfma_f32_16x16x32_bf16 v[172:175], v[160:163], v[92:95], v[172:175]\n"
"ds_read_b64_tr_b16 v[160:161], v253 offset:2048\n"
"v_mfma_f32_16x16x32_bf16 v[176:179], v[164:167], v[88:91], v[176:179]\n"
"ds_read_b64_tr_b16 v[162:163], v253 offset:2560\n"
"v_mfma_f32_16x16x32_bf16 v[180:183], v[164:167], v[92:95], v[180:183]\n"
"ds_read_b64_tr_b16 v[164:165], v254 offset:2048\n"
"ds_read_b64_tr_b16 v[166:167], v254 offset:2560\n"
"buffer_load_dwordx4 v250, s[24:27], s68 offen lds\n"
"s_waitcnt lgkmcnt(8)\n"
"v_mfma_f32_16x16x32_bf16 v[168:171], v[144:147], v[96:99], v[168:171]\n"
"v_mfma_f32_16x16x32_bf16 v[172:175], v[144:147], v[100:103], v[172:175]\n"
"ds_read_b64_tr_b16 v[144:145], v253 offset:3072\n"
"v_mfma_f32_16x16x32_bf16 v[176:179], v[148:151], v[96:99], v[176:179]\n"
"ds_read_b64_tr_b16 v[146:147], v253 offset:3584\n"
"v_mfma_f32_16x16x32_bf16 v[180:183], v[148:151], v[100:103], v[180:183]\n"
"ds_read_b64_tr_b16 v[148:149], v254 offset:3072\n"
"buffer_load_dwordx4 v250, s[24:27], s69 offen offset:1024 lds\n"
"s_waitcnt lgkmcnt(7)\n"
"v_mfma_f32_16x16x32_bf16 v[168:171], v[152:155], v[104:107], v[168:171]\n"
"ds_read_b64_tr_b16 v[150:151], v254 offset:3584\n"
"v_mfma_f32_16x16x32_bf16 v[172:175], v[152:155], v[108:111], v[172:175]\n"
"ds_read_b64_tr_b16 v[152:153], v253 offset:4096\n"
"v_mfma_f32_16x16x32_bf16 v[176:179], v[156:159], v[104:107], v[176:179]\n"
"ds_read_b64_tr_b16 v[154:155], v253 offset:4608\n"
"v_mfma_f32_16x16x32_bf16 v[180:183], v[156:159], v[108:111], v[180:183]\n"
"ds_read_b64_tr_b16 v[156:157], v254 offset:4096\n"
"buffer_load_dwordx4 v250, s[24:27], s70 offen offset:2048 lds\n"
"s_waitcnt lgkmcnt(7)\n"
"v_mfma_f32_16x16x32_bf16 v[168:171], v[160:163], v[112:115], v[168:171]\n"
"ds_read_b64_tr_b16 v[158:159], v254 offset:4608\n"
"v_mfma_f32_16x16x32_bf16 v[172:175], v[160:163], v[116:119], v[172:175]\n"
"ds_read_b64_tr_b16 v[160:161], v253 offset:5120\n"
"v_mfma_f32_16x16x32_bf16 v[176:179], v[164:167], v[112:115], v[176:179]\n"
"ds_read_b64_tr_b16 v[162:163], v253 offset:5632\n"
"v_mfma_f32_16x16x32_bf16 v[180:183], v[164:167], v[116:119], v[180:183]\n"
"ds_read_b64_tr_b16 v[164:165], v254 offset:5120\n"
"ds_read_b64_tr_b16 v[166:167], v254 offset:5632\n"
"buffer_load_dwordx4 v250, s[24:27], s71 offen offset:3072 lds\n"
"v_add_u32 v253, s44, v252\n"
"v_xor_b32 v254, 32, v253\n"
"s_waitcnt lgkmcnt(8)\n"
"v_mfma_f32_16x16x32_bf16 v[168:171], v[144:147], v[120:123], v[168:171]\n"
"v_mfma_f32_16x16x32_bf16 v[172:175], v[144:147], v[124:127], v[172:175]\n"
"s_add_u32 m0, m0, 4096\n"
"s_waitcnt vmcnt(18)\n"
"s_barrier\n"
"ds_read_b64_tr_b16 v[144:145], v253 offset:0\n"
"v_mfma_f32_16x16x32_bf16 v[176:179], v[148:151], v[120:123], v[176:179]\n"
"ds_read_b64_tr_b16 v[146:147], v253 offset:512\n"
"v_mfma_f32_16x16x32_bf16 v[180:183], v[148:151], v[124:127], v[180:183]\n"
"ds_read_b64_tr_b16 v[148:149], v254 offset:0\n"
"buffer_load_dwordx4 v250, s[24:27], s72 offen lds\n"
"s_waitcnt lgkmcnt(7)\n"
"v_mfma_f32_16x16x32_bf16 v[168:171], v[152:155], v[128:131], v[168:171]\n"
"ds_read_b64_tr_b16 v[150:151], v254 offset:512\n"
"s_add_u32 s42, s44, 24576\n"
"v_mfma_f32_16x16x32_bf16 v[172:175], v[152:155], v[132:135], v[172:175]\n"
"ds_read_b64_tr_b16 v[152:153], v253 offset:1024\n"
"s_cmp_eq_u32 s42, 122880\n"
"v_mfma_f32_16x16x32_bf16 v[176:179], v[156:159], v[128:131], v[176:179]\n"
"ds_read_b64_tr_b16 v[154:155], v253 offset:1536\n"
"s_cselect_b32 s42, 0, s42\n"
"v_mfma_f32_16x16x32_bf16 v[180:183], v[156:159], v[132:135], v[180:183]\n"
"ds_read_b64_tr_b16 v[156:157], v254 offset:1024\n"
"buffer_load_dwordx4 v250, s[24:27], s73 offen offset:1024 lds\n"
"s_waitcnt lgkmcnt(7)\n"
"v_mfma_f32_16x16x32_bf16 v[184:187], v[160:163], v[136:139], v[168:171]\n"
"ds_read_b64_tr_b16 v[158:159], v254 offset:1536\n"
"s_add_u32 m0, s43, s57\n"
"v_mfma_f32_16x16x32_bf16 v[188:191], v[160:163], v[140:143], v[172:175]\n"
"ds_read_b64_tr_b16 v[160:161], v253 offset:2048\n"
"s_sub_u32 s45, s45, 1\n"
"v_mfma_f32_16x16x32_bf16 v[192:195], v[164:167], v[136:139], v[176:179]\n"
"ds_read_b64_tr_b16 v[162:163], v253 offset:2560\n"
"s_cmp_lg_u32 s45, 0\n"
"v_mfma_f32_16x16x32_bf16 v[196:199], v[164:167], v[140:143], v[180:183]\n"
"ds_read_b64_tr_b16 v[164:165], v254 offset:2048\n"
"ds_read_b64_tr_b16 v[166:167], v254 offset:2560\n"
"buffer_load_dwordx4 v250, s[24:27], s74 offen lds\n"
"s_cbranch_scc1 LQD_%=\n"
"s_mov_b32 s45, 3\n"
"s_lshl_b32 s35, s34, 5\n"
"LSD_%=:\n"
"s_waitcnt lgkmcnt(8)\n"
"v_mfma_f32_16x16x32_bf16 v[168:171], v[144:147], v[0:3], 0\n"
"v_mfma_f32_16x16x32_bf16 v[172:175], v[144:147], v[4:7], 0\n"
"ds_read_b64_tr_b16 v[144:145], v253 offset:3072\n"
"v_mfma_f32_16x16x32_bf16 v[176:179], v[148:151], v[0:3], 0\n"
"ds_read_b64_tr_b16 v[146:147], v253 offset:3584\n"
"v_mfma_f32_16x16x32_bf16 v[180:183], v[148:151], v[4:7], 0\n"
"ds_read_b64_tr_b16 v[148:149], v254 offset:3072\n"
"buffer_load_dwordx4 v250, s[24:27], s75 offen offset:1024 lds\n"
"v_mul_f32 v200, s56, v184\n"
"v_exp_f32 v200, v200\n"
"v_mul_f32 v201, s56, v185\n"
"v_add_f32 v200, 1.0, v200\n"
"v_exp_f32 v201, v201\n"
"v_mul_f32 v202, s56, v186\n"
"s_waitcnt lgkmcnt(7)\n"
"v_mfma_f32_16x16x32_bf16 v[168:171], v[152:155], v[8:11], v[168:171]\n"
"ds_read_b64_tr_b16 v[150:151], v254 offset:3584\n"
"v_mfma_f32_16x16x32_bf16 v[172:175], v[152:155], v[12:15], v[172:175]\n"
"ds_read_b64_tr_b16 v[152:153], v253 offset:4096\n"
"v_mfma_f32_16x16x32_bf16 v[176:179], v[156:159], v[8:11], v[176:179]\n"
"ds_read_b64_tr_b16 v[154:155], v253 offset:4608\n"
"v_mfma_f32_16x16x32_bf16 v[180:183], v[156:159], v[12:15], v[180:183]\n"
"ds_read_b64_tr_b16 v[156:157], v254 offset:4096\n"
"buffer_load_dwordx4 v250, s[24:27], s76 offen offset:2048 lds\n"
"v_rcp_f32 v200, v200\n"
"v_add_f32 v201, 1.0, v201\n"
"v_mul_f32 v184, v184, v200\n"
"v_exp_f32 v202, v202\n"
"v_mul_f32 v200, s56, v187\n"
"v_rcp_f32 v201, v201\n"
"s_waitcnt lgkmcnt(7)\n"
"v_mfma_f32_16x16x32_bf16 v[168:171], v[160:163], v[16:19], v[168:171]\n"
"ds_read_b64_tr_b16 v[158:159], v254 offset:4608\n"
"v_mfma_f32_16x16x32_bf16 v[172:175], v[160:163], v[20:23], v[172:175]\n"
"ds_read_b64_tr_b16 v[160:161], v253 offset:5120\n"
"v_mfma_f32_16x16x32_bf16 v[176:179], v[164:167], v[16:19], v[176:179]\n"
"ds_read_b64_tr_b16 v[162:163], v253 offset:5632\n"
"v_mfma_f32_16x16x32_bf16 v[180:183], v[164:167], v[20:23], v[180:183]\n"
"ds_read_b64_tr_b16 v[164:165], v254 offset:5120\n"
"ds_read_b64_tr_b16 v[166:167], v254 offset:5632\n"
"buffer_load_dwordx4 v250, s[24:27], s77 offen offset:3072 lds\n"
"v_add_u32 v253, s42, v252\n"
"v_xor_b32 v254, 32, v253\n"
"v_add_f32 v202, 1.0, v202\n"
"v_mul_f32 v185, v185, v201\n"
"v_cvt_pk_bf16_f32 v208, v184, v185\n"
"v_exp_f32 v200, v200\n"
"s_waitcnt lgkmcnt(8)\n"
"v_mfma_f32_16x16x32_bf16 v[168:171], v[144:147], v[24:27], v[168:171]\n"
"v_mfma_f32_16x16x32_bf16 v[172:175], v[144:147], v[28:31], v[172:175]\n"
"s_add_u32 m0, m0, 4096\n"
"s_waitcnt vmcnt(16)\n"
"s_barrier\n"
"ds_read_b64_tr_b16 v[144:145], v253 offset:0\n"
"v_mfma_f32_16x16x32_bf16 v[176:179], v[148:151], v[24:27], v[176:179]\n"
"ds_read_b64_tr_b16 v[146:147], v253 offset:512\n"
"v_mfma_f32_16x16x32_bf16 v[180:183], v[148:151], v[28:31], v[180:183]\n"
"ds_read_b64_tr_b16 v[148:149], v254 offset:0\n"
"buffer_load_dwordx4 v250, s[24:27], s78 offen lds\n"
"v_mul_f32 v201, s56, v192\n"
"v_rcp_f32 v202, v202\n"
"v_add_f32 v200, 1.0, v200\n"
"v_mul_f32 v186, v186, v202\n"
"v_exp_f32 v201, v201\n"
"v_mul_f32 v202, s56, v193\n"
"s_waitcnt lgkmcnt(7)\n"
"v_mfma_f32_16x16x32_bf16 v[168:171], v[152:155], v[32:35], v[168:171]\n"
"ds_read_b64_tr_b16 v[150:151], v254 offset:512\n"
"s_add_u32 s43, s42, 24576\n"
"v_mfma_f32_16x16x32_bf16 v[172:175], v[152:155], v[36:39], v[172:175]\n"
"ds_read_b64_tr_b16 v[152:153], v253 offset:1024\n"
"s_cmp_eq_u32 s43, 122880\n"
"v_mfma_f32_16x16x32_bf16 v[176:179], v[156:159], v[32:35], v[176:179]\n"
"ds_read_b64_tr_b16 v[154:155], v253 offset:1536\n"
"s_cselect_b32 s43, 0, s43\n"
"v_mfma_f32_16x16x32_bf16 v[180:183], v[156:159], v[36:39], v[180:183]\n"
"ds_read_b64_tr_b16 v[156:157], v254 offset:1024\n"
"buffer_load_dwordx4 v250, s[24:27], s79 offen offset:1024 lds\n"
"v_rcp_f32 v200, v200\n"
"v_add_f32 v201, 1.0, v201\n"
"v_mul_f32 v187, v187, v200\n"
"v_cvt_pk_bf16_f32 v209, v186, v187\n"
"v_exp_f32 v202, v202\n"
"s_waitcnt lgkmcnt(7)\n"
"v_mfma_f32_16x16x32_bf16 v[168:171], v[160:163], v[40:43], v[168:171]\n"
"ds_read_b64_tr_b16 v[158:159], v254 offset:1536\n"
"s_add_u32 m0, s44, s57\n"
"v_mfma_f32_16x16x32_bf16 v[172:175], v[160:163], v[44:47], v[172:175]\n"
"ds_read_b64_tr_b16 v[160:161], v253 offset:2048\n"
"v_mfma_f32_16x16x32_bf16 v[176:179], v[164:167], v[40:43], v[176:179]\n"
"ds_read_b64_tr_b16 v[162:163], v253 offset:2560\n"
"v_mfma_f32_16x16x32_bf16 v[180:183], v[164:167], v[44:47], v[180:183]\n"
"ds_read_b64_tr_b16 v[164:165], v254 offset:2048\n"
"ds_read_b64_tr_b16 v[166:167], v254 offset:2560\n"
"buffer_load_dwordx4 v250, s[24:27], s80 offen lds\n"
"v_mul_f32 v200, s56, v194\n"
"v_rcp_f32 v201, v201\n"
"v_add_f32 v202, 1.0, v202\n"
"v_mul_f32 v192, v192, v201\n"
"v_exp_f32 v200, v200\n"
"v_mul_f32 v201, s56, v195\n"
"s_waitcnt lgkmcnt(8)\n"
"v_mfma_f32_16x16x32_bf16 v[168:171], v[144:147], v[48:51], v[168:171]\n"
"v_mfma_f32_16x16x32_bf16 v[172:175], v[144:147], v[52:55], v[172:175]\n"
"ds_read_b64_tr_b16 v[144:145], v253 offset:3072\n"
"v_mfma_f32_16x16x32_bf16 v[176:179], v[148:151], v[48:51], v[176:179]\n"
"ds_read_b64_tr_b16 v[146:147], v253 offset:3584\n"
"v_mfma_f32_16x16x32_bf16 v[180:183], v[148:151], v[52:55], v[180:183]\n"
"ds_read_b64_tr_b16 v[148:149], v254 offset:3072\n"
"buffer_load_dwordx4 v250, s[24:27], s81 offen offset:1024 lds\n"
"v_rcp_f32 v202, v202\n"
"v_add_f32 v200, 1.0, v200\n"
"v_mul_f32 v193, v193, v202\n"
"v_cvt_pk_bf16_f32 v210, v192, v193\n"
"v_exp_f32 v201, v201\n"
"v_mul_f32 v202, s56, v188\n"
"s_waitcnt lgkmcnt(7)\n"
"v_mfma_f32_16x16x32_bf16 v[168:171], v[152:155], v[56:59], v[168:171]\n"
"ds_read_b64_tr_b16 v[150:151], v254 offset:3584\n"
"v_mfma_f32_16x16x32_bf16 v[172:175], v[152:155], v[60:63], v[172:175]\n"
"ds_read_b64_tr_b16 v[152:153], v253 offset:4096\n"
"v_mfma_f32_16x16x32_bf16 v[176:179], v[156:159], v[56:59], v[176:179]\n"
"ds_read_b64_tr_b16 v[154:155], v253 offset:4608\n"
"v_mfma_f32_16x16x32_bf16 v[180:183], v[156:159], v[60:63], v[180:183]\n"
"ds_read_b64_tr_b16 v[156:157], v254 offset:4096\n"
"buffer_load_dwordx4 v250, s[24:27], s82 offen offset:2048 lds\n"
"v_rcp_f32 v200, v200\n"
"v_add_f32 v201, 1.0, v201\n"
"v_mul_f32 v194, v194, v200\n"
"v_exp_f32 v202, v202\n"
"v_mul_f32 v200, s56, v189\n"
"v_rcp_f32 v201, v201\n"
"s_waitcnt lgkmcnt(7)\n"
"v_mfma_f32_16x16x32_bf16 v[168:171], v[160:163], v[64:67], v[168:171]\n"
"ds_read_b64_tr_b16 v[158:159], v254 offset:4608\n"
"v_mfma_f32_16x16x32_bf16 v[172:175], v[160:163], v[68:71], v[172:175]\n"
"ds_read_b64_tr_b16 v[160:161], v253 offset:5120\n"
"v_mfma_f32_16x16x32_bf16 v[176:179], v[164:167], v[64:67], v[176:179]\n"
"ds_read_b64_tr_b16 v[162:163], v253 offset:5632\n"
"v_mfma_f32_16x16x32_bf16 v[180:183], v[164:167], v[68:71], v[180:183]\n"
"ds_read_b64_tr_b16 v[164:165], v254 offset:5120\n"
"ds_read_b64_tr_b16 v[166:167], v254 offset:5632\n"
"buffer_load_dwordx4 v250, s[24:27], s83 offen offset:3072 lds\n"
"v_add_u32 v253, s43, v252\n"
"v_xor_b32 v254, 32, v253\n"
"v_add_f32 v202, 1.0, v202\n"
"v_mul_f32 v195, v195, v201\n"
"v_cvt_pk_bf16_f32 v211, v194, v195\n"
"ds_write_b128 v249, v[208:211]\n"
"s_waitcnt lgkmcnt(9)\n"
"v_mfma_f32_16x16x32_bf16 v[168:171], v[144:147], v[72:75], v[168:171]\n"
"v_mfma_f32_16x16x32_bf16 v[172:175], v[144:147], v[76:79], v[172:175]\n"
"s_add_u32 m0, m0, 4096\n"
"s_waitcnt vmcnt(16)\n"
"s_barrier\n"
"ds_read_b64_tr_b16 v[144:145], v253 offset:0\n"
"v_mfma_f32_16x16x32_bf16 v[176:179], v[148:151], v[72:75], v[176:179]\n"
"ds_read_b64_tr_b16 v[146:147], v253 offset:512\n"
"v_mfma_f32_16x16x32_bf16 v[180:183], v[148:151], v[76:79], v[180:183]\n"
"ds_read_b64_tr_b16 v[148:149], v254 offset:0\n"
"buffer_load_dwordx4 v250, s[24:27], s84 offen lds\n"
"v_exp_f32 v200, v200\n"
"v_mul_f32 v201, s56, v190\n"
"v_rcp_f32 v202, v202\n"
"v_add_f32 v200, 1.0, v200\n"
"v_mul_f32 v188, v188, v202\n"
"v_exp_f32 v201, v201\n"
"s_waitcnt lgkmcnt(8)\n"
"v_mfma_f32_16x16x32_bf16 v[168:171], v[152:155], v[80:83], v[168:171]\n"
"ds_read_b64_tr_b16 v[150:151], v254 offset:512\n"
"s_add_u32 s44, s43, 24576\n"
"v_mfma_f32_16x16x32_bf16 v[172:175], v[152:155], v[84:87], v[172:175]\n"
"ds_read_b64_tr_b16 v[152:153], v253 offset:1024\n"
"s_cmp_eq_u32 s44, 122880\n"
"v_mfma_f32_16x16x32_bf16 v[176:179], v[156:159], v[80:83], v[176:179]\n"
"ds_read_b64_tr_b16 v[154:155], v253 offset:1536\n"
"s_cselect_b32 s44, 0, s44\n"
"v_mfma_f32_16x16x32_bf16 v[180:183], v[156:159], v[84:87], v[180:183]\n"
"ds_read_b64_tr_b16 v[156:157], v254 offset:1024\n"
"buffer_load_dwordx4 v250, s[24:27], s85 offen offset:1024 lds\n"
"v_add_u32 v250, s40, v250\n"
"s_cmp_eq_u32 s45, 1\n"
"s_cbranch_scc1 O6_%=\n"
"E5_%=:\n"
"v_mul_f32 v202, s56, v191\n"
"v_rcp_f32 v200, v200\n"
"v_add_f32 v201, 1.0, v201\n"
"v_mul_f32 v189, v189, v200\n"
"v_cvt_pk_bf16_f32 v212, v188, v189\n"
"s_waitcnt lgkmcnt(8)\n"
"v_mfma_f32_16x16x32_bf16 v[168:171], v[160:163], v[88:91], v[168:171]\n"
"ds_read_b64_tr_b16 v[158:159], v254 offset:1536\n"
"s_add_u32 m0, s42, s57\n"
"v_mfma_f32_16x16x32_bf16 v[172:175], v[160:163], v[92:95], v[172:175]\n"
"ds_read_b64_tr_b16 v[160:161], v253 offset:2048\n"
"v_mfma_f32_16x16x32_bf16 v[176:179], v[164:167], v[88:91], v[176:179]\n"
"ds_read_b64_tr_b16 v[162:163], v253 offset:2560\n"
"v_mfma_f32_16x16x32_bf16 v[180:183], v[164:167], v[92:95], v[180:183]\n"
"ds_read_b64_tr_b16 v[164:165], v254 offset:2048\n"
"ds_read_b64_tr_b16 v[166:167], v254 offset:2560\n"
"buffer_load_dwordx4 v250, s[24:27], s68 offen sc1 lds\n"
"v_exp_f32 v202, v202\n"
"v_mul_f32 v200, s56, v196\n"
"v_rcp_f32 v201, v201\n"
"v_add_f32 v202, 1.0, v202\n"
"v_mul_f32 v190, v190, v201\n"
"v_exp_f32 v200, v200\n"
"s_waitcnt lgkmcnt(8)\n"
"v_mfma_f32_16x16x32_bf16 v[168:171], v[144:147], v[96:99], v[168:171]\n"
"v_mfma_f32_16x16x32_bf16 v[172:175], v[144:147], v[100:103], v[172:175]\n"
"ds_read_b64_tr_b16 v[144:145], v253 offset:3072\n"
"v_mfma_f32_16x16x32_bf16 v[176:179], v[148:151], v[96:99], v[176:179]\n"
"ds_read_b64_tr_b16 v[146:147], v253 offset:3584\n"
"v_mfma_f32_16x16x32_bf16 v[180:183], v[148:151], v[100:103], v[180:183]\n"
"ds_read_b64_tr_b16 v[148:149], v254 offset:3072\n"
"buffer_load_dwordx4 v250, s[24:27], s69 offen offset:1024 sc1 lds\n"
"v_mul_f32 v201, s56, v197\n"
"v_rcp_f32 v202, v202\n"
"v_add_f32 v200, 1.0, v200\n"
"v_mul_f32 v191, v191, v202\n"
"v_cvt_pk_bf16_f32 v213, v190, v191\n"
"s_waitcnt lgkmcnt(7)\n"
"v_mfma_f32_16x16x32_bf16 v[168:171], v[152:155], v[104:107], v[168:171]\n"
"ds_read_b64_tr_b16 v[150:151], v254 offset:3584\n"
"s_cmp_eq_u32 s45, 1\n"
"v_mfma_f32_16x16x32_bf16 v[172:175], v[152:155], v[108:111], v[172:175]\n"
"ds_read_b64_tr_b16 v[152:153], v253 offset:4096\n"
"s_cselect_b32 s60, s35, 0\n"
"v_mfma_f32_16x16x32_bf16 v[176:179], v[156:159], v[104:107], v[176:179]\n"
"ds_read_b64_tr_b16 v[154:155], v253 offset:4608\n"
"v_mfma_f32_16x16x32_bf16 v[180:183], v[156:159], v[108:111], v[180:183]\n"
"ds_read_b64_tr_b16 v[156:157], v254 offset:4096\n"
"buffer_load_dwordx4 v250, s[24:27], s70 offen offset:2048 sc1 lds\n"
"v_xor_b32 v252, s60, v252\n"
"v_exp_f32 v201, v201\n"
"v_mul_f32 v202, s56, v198\n"
"v_rcp_f32 v200, v200\n"
"v_add_f32 v201, 1.0, v201\n"
"v_mul_f32 v196, v196, v200\n"
"s_waitcnt lgkmcnt(7)\n"
"v_mfma_f32_16x16x32_bf16 v[168:171], v[160:163], v[112:115], v[168:171]\n"
"ds_read_b64_tr_b16 v[158:159], v254 offset:4608\n"
"v_mfma_f32_16x16x32_bf16 v[172:175], v[160:163], v[116:119], v[172:175]\n"
"ds_read_b64_tr_b16 v[160:161], v253 offset:5120\n"
"v_mfma_f32_16x16x32_bf16 v[176:179], v[164:167], v[112:115], v[176:179]\n"
"ds_read_b64_tr_b16 v[162:163], v253 offset:5632\n"
"v_mfma_f32_16x16x32_bf16 v[180:183], v[164:167], v[116:119], v[180:183]\n"
"ds_read_b64_tr_b16 v[164:165], v254 offset:5120\n"
"ds_read_b64_tr_b16 v[166:167], v254 offset:5632\n"
"buffer_load_dwordx4 v250, s[24:27], s71 offen offset:3072 sc1 lds\n"
"v_add_u32 v253, s44, v252\n"
"v_xor_b32 v254, 32, v253\n"
"v_exp_f32 v202, v202\n"
"v_mul_f32 v200, s56, v199\n"
"v_rcp_f32 v201, v201\n"
"v_add_f32 v202, 1.0, v202\n"
"s_waitcnt lgkmcnt(8)\n"
"v_mfma_f32_16x16x32_bf16 v[168:171], v[144:147], v[120:123], v[168:171]\n"
"v_mfma_f32_16x16x32_bf16 v[172:175], v[144:147], v[124:127], v[172:175]\n"
"s_add_u32 m0, m0, 4096\n"
"s_waitcnt vmcnt(16)\n"
"s_barrier\n"
"ds_read_b64_tr_b16 v[144:145], v253 offset:0\n"
"v_mfma_f32_16x16x32_bf16 v[176:179], v[148:151], v[120:123], v[176:179]\n"
"ds_read_b64_tr_b16 v[146:147], v253 offset:512\n"
"v_mfma_f32_16x16x32_bf16 v[180:183], v[148:151], v[124:127], v[180:183]\n"
"ds_read_b64_tr_b16 v[148:149], v254 offset:0\n"
"buffer_load_dwordx4 v250, s[24:27], s72 offen sc1 lds\n"
"v_mul_f32 v197, v197, v201\n"
"v_cvt_pk_bf16_f32 v214, v196, v197\n"
"v_exp_f32 v200, v200\n"
"v_rcp_f32 v202, v202\n"
"v_add_f32 v200, 1.0, v200\n"
"v_mul_f32 v198, v198, v202\n"
"s_waitcnt lgkmcnt(7)\n"
"v_mfma_f32_16x16x32_bf16 v[168:171], v[152:155], v[128:131], v[168:171]\n"
"ds_read_b64_tr_b16 v[150:151], v254 offset:512\n"
"s_add_u32 s42, s44, 24576\n"
"v_mfma_f32_16x16x32_bf16 v[172:175], v[152:155], v[132:135], v[172:175]\n"
"ds_read_b64_tr_b16 v[152:153], v253 offset:1024\n"
"s_cmp_eq_u32 s42, 122880\n"
"v_mfma_f32_16x16x32_bf16 v[176:179], v[156:159], v[128:131], v[176:179]\n"
"ds_read_b64_tr_b16 v[154:155], v253 offset:1536\n"
"s_cselect_b32 s42, 0, s42\n"
"v_mfma_f32_16x16x32_bf16 v[180:183], v[156:159], v[132:135], v[180:183]\n"
"ds_read_b64_tr_b16 v[156:157], v254 offset:1024\n"
"buffer_load_dwordx4 v250, s[24:27], s73 offen offset:1024 sc1 lds\n"
"v_rcp_f32 v200, v200\n"
"s_nop 0\n"
"v_mul_f32 v199, v199, v200\n"
"v_cvt_pk_bf16_f32 v215, v198, v199\n"
"ds_write_b128 v249, v[212:215] offset:1024\n"
"v_add_u32 v249, 0x2000, v249\n"
"s_waitcnt lgkmcnt(8)\n"
"v_mfma_f32_16x16x32_bf16 v[184:187], v[160:163], v[136:139], v[168:171]\n"
"ds_read_b64_tr_b16 v[158:159], v254 offset:1536\n"
"s_add_u32 m0, s43, s57\n"
"v_mfma_f32_16x16x32_bf16 v[188:191], v[160:163], v[140:143], v[172:175]\n"
"ds_read_b64_tr_b16 v[160:161], v253 offset:2048\n"
"s_sub_u32 s45, s45, 1\n"
"v_mfma_f32_16x16x32_bf16 v[192:195], v[164:167], v[136:139], v[176:179]\n"
"ds_read_b64_tr_b16 v[162:163], v253 offset:2560\n"
"s_cmp_lg_u32 s45, 0\n"
"v_mfma_f32_16x16x32_bf16 v[196:199], v[164:167], v[140:143], v[180:183]\n"
"ds_read_b64_tr_b16 v[164:165], v254 offset:2048\n"
"ds_read_b64_tr_b16 v[166:167], v254 offset:2560\n"
"buffer_load_dwordx4 v250, s[24:27], s74 offen sc1 lds\n"
"s_cbranch_scc1 LSD_%=\n"
"s_lshl_b32 s60, s48, 5\n"
"s_add_u32 s60, s60, s47\n"
"s_mul_i32 s60, s60, 9216\n"
"s_add_u32 s60, s60, 65536\n"
"buffer_load_dwordx4 v[176:179], v224, s[28:31], s60 offen sc1\n"
"v_mov_b32 v180, 0\n"
"v_mov_b32 v181, 0\n"
"v_mov_b32 v182, 0\n"
"v_mov_b32 v183, 0\n"
"s_mov_b32 exec_hi, 0\n"
"buffer_load_dwordx4 v[180:183], v226, s[28:31], s60 offen sc1\n"
"s_mov_b32 exec_hi, -1\n"
"s_waitcnt lgkmcnt(11)\n"
"v_mfma_f32_16x16x32_bf16 v[168:171], v[144:147], v[0:3], 0\n"
"v_mfma_f32_16x16x32_bf16 v[172:175], v[144:147], v[4:7], 0\n"
"ds_read_b64_tr_b16 v[144:145], v253 offset:3072\n"
"buffer_load_dwordx4 v255, s[20:23], m0 offen offset:1024 sc1 lds\n"
"s_waitcnt lgkmcnt(8)\n"
"v_mfma_f32_16x16x32_bf16 v[168:171], v[152:155], v[8:11], v[168:171]\n"
"ds_read_b64_tr_b16 v[146:147], v253 offset:3584\n"
"v_mfma_f32_16x16x32_bf16 v[172:175], v[152:155], v[12:15], v[172:175]\n"
"ds_read_b64_tr_b16 v[152:153], v253 offset:4096\n"
"buffer_load_dwordx4 v255, s[20:23], m0 offen offset:2048 sc1 lds\n"
"v_mul_f32 v200, s56, v184\n"
"v_exp_f32 v200, v200\n"
"v_mul_f32 v201, s56, v185\n"
"v_add_f32 v200, 1.0, v200\n"
"v_exp_f32 v201, v201\n"
"s_waitcnt lgkmcnt(5)\n"
"v_mfma_f32_16x16x32_bf16 v[168:171], v[160:163], v[16:19], v[168:171]\n"
"ds_read_b64_tr_b16 v[154:155], v253 offset:4608\n"
"v_mfma_f32_16x16x32_bf16 v[172:175], v[160:163], v[20:23], v[172:175]\n"
"ds_read_b64_tr_b16 v[160:161], v253 offset:5120\n"
"ds_read_b64_tr_b16 v[162:163], v253 offset:5632\n"
"buffer_load_dwordx4 v255, s[20:23], m0 offen offset:3072 sc1 lds\n"
"v_add_u32 v253, s42, v252\n"
"v_mul_f32 v202, s56, v186\n"
"v_rcp_f32 v200, v200\n"
"v_add_f32 v201, 1.0, v201\n"
"v_mul_f32 v184, v184, v200\n"
"v_exp_f32 v202, v202\n"
"v_mul_f32 v200, s56, v187\n"
"s_waitcnt lgkmcnt(4)\n"
"v_mfma_f32_16x16x32_bf16 v[168:171], v[144:147], v[24:27], v[168:171]\n"
"v_mfma_f32_16x16x32_bf16 v[172:175], v[144:147], v[28:31], v[172:175]\n"
"s_add_u32 m0, m0, 4096\n"
"s_waitcnt vmcnt(18)\n"
"s_barrier\n"
"ds_read_b64_tr_b16 v[144:145], v253 offset:0\n"
"s_add_u32 s43, s42, 24576\n"
"buffer_load_dwordx4 v255, s[20:23], m0 offen sc1 lds\n"
"buffer_load_dwordx4 v255, s[20:23], m0 offen offset:1024 sc1 lds\n"
"v_rcp_f32 v201, v201\n"
"v_add_f32 v202, 1.0, v202\n"
"v_mul_f32 v185, v185, v201\n"
"v_cvt_pk_bf16_f32 v208, v184, v185\n"
"v_exp_f32 v200, v200\n"
"v_mul_f32 v201, s56, v192\n"
"s_waitcnt lgkmcnt(3)\n"
"v_mfma_f32_16x16x32_bf16 v[168:171], v[152:155], v[32:35], v[168:171]\n"
"ds_read_b64_tr_b16 v[146:147], v253 offset:512\n"
"s_add_u32 m0, s57, 49152\n"
"v_mfma_f32_16x16x32_bf16 v[172:175], v[152:155], v[36:39], v[172:175]\n"
"ds_read_b64_tr_b16 v[152:153], v253 offset:1024\n"
"s_cmp_eq_u32 s43, 122880\n"
"s_cselect_b32 s43, 0, s43\n"
"buffer_load_dwordx4 v255, s[20:23], m0 offen sc1 lds\n"
"buffer_load_dwordx4 v255, s[20:23], m0 offen offset:1024 sc1 lds\n"
"v_bfe_u32 v241, v255, 2, 7\n"
"v_and_b32 v240, 63, v241\n"
"s_lshl_b32 s60, s47, 9\n"
"s_add_u32 s60, s60, 32768\n"
"s_lshl_b32 s61, s94, 7\n"
"s_add_u32 s61, s61, s60\n"
"buffer_load_dword v245, v241, s[28:31], s61 offen sc1\n"
"buffer_load_dword v246, v240, s[28:31], s60 offen offset:256 sc1\n"
"buffer_load_dword v247, v240, s[28:31], s60 offen offset:320 sc1\n"
"v_rcp_f32 v202, v202\n"
"v_add_f32 v200, 1.0, v200\n"
"v_mul_f32 v186, v186, v202\n"
"v_exp_f32 v201, v201\n"
"v_mul_f32 v202, s56, v193\n"
"v_rcp_f32 v200, v200\n"
"s_waitcnt lgkmcnt(3)\n"
"v_mfma_f32_16x16x32_bf16 v[168:171], v[160:163], v[40:43], v[168:171]\n"
"ds_read_b64_tr_b16 v[154:155], v253 offset:1536\n"
"v_mfma_f32_16x16x32_bf16 v[172:175], v[160:163], v[44:47], v[172:175]\n"
"ds_read_b64_tr_b16 v[160:161], v253 offset:2048\n"
"ds_read_b64_tr_b16 v[162:163], v253 offset:2560\n"
"buffer_load_dwordx4 v255, s[20:23], m0 offen offset:2048 sc1 lds\n"
"buffer_load_dwordx4 v255, s[20:23], m0 offen offset:3072 sc1 lds\n"
"v_add_f32 v201, 1.0, v201\n"
"v_mul_f32 v187, v187, v200\n"
"v_cvt_pk_bf16_f32 v209, v186, v187\n"
"v_exp_f32 v202, v202\n"
"v_mul_f32 v200, s56, v194\n"
"s_waitcnt lgkmcnt(4)\n"
"v_mfma_f32_16x16x32_bf16 v[168:171], v[144:147], v[48:51], v[168:171]\n"
"v_mfma_f32_16x16x32_bf16 v[172:175], v[144:147], v[52:55], v[172:175]\n"
"ds_read_b64_tr_b16 v[144:145], v253 offset:3072\n"
"buffer_load_dwordx2 v[236:237], off, s[28:31], s100 sc1\n"
"v_rcp_f32 v201, v201\n"
"v_add_f32 v202, 1.0, v202\n"
"v_mul_f32 v192, v192, v201\n"
"v_exp_f32 v200, v200\n"
"v_mul_f32 v201, s56, v195\n"
"v_rcp_f32 v202, v202\n"
"s_waitcnt lgkmcnt(3)\n"
"v_mfma_f32_16x16x32_bf16 v[168:171], v[152:155], v[56:59], v[168:171]\n"
"ds_read_b64_tr_b16 v[146:147], v253 offset:3584\n"
"v_mfma_f32_16x16x32_bf16 v[172:175], v[152:155], v[60:63], v[172:175]\n"
"ds_read_b64_tr_b16 v[152:153], v253 offset:4096\n"
"v_add_f32 v200, 1.0, v200\n"
"v_mul_f32 v193, v193, v202\n"
"v_cvt_pk_bf16_f32 v210, v192, v193\n"
"v_exp_f32 v201, v201\n"
"v_mul_f32 v202, s56, v188\n"
"v_rcp_f32 v200, v200\n"
"s_waitcnt lgkmcnt(3)\n"
"v_mfma_f32_16x16x32_bf16 v[168:171], v[160:163], v[64:67], v[168:171]\n"
"ds_read_b64_tr_b16 v[154:155], v253 offset:4608\n"
"v_mfma_f32_16x16x32_bf16 v[172:175], v[160:163], v[68:71], v[172:175]\n"
"ds_read_b64_tr_b16 v[160:161], v253 offset:5120\n"
"ds_read_b64_tr_b16 v[162:163], v253 offset:5632\n"
"v_add_u32 v253, s43, v252\n"
"v_add_f32 v201, 1.0, v201\n"
"v_mul_f32 v194, v194, v200\n"
"v_exp_f32 v202, v202\n"
"v_mul_f32 v200, s56, v189\n"
"v_rcp_f32 v201, v201\n"
"v_add_f32 v202, 1.0, v202\n"
"s_waitcnt lgkmcnt(4)\n"
"v_mfma_f32_16x16x32_bf16 v[168:171], v[144:147], v[72:75], v[168:171]\n"
"v_mfma_f32_16x16x32_bf16 v[172:175], v[144:147], v[76:79], v[172:175]\n"
"s_add_u32 m0, m0, 4096\n"
"s_waitcnt vmcnt(22)\n"
"s_barrier\n"
"ds_read_b64_tr_b16 v[144:145], v253 offset:0\n"
"buffer_load_dwordx4 v255, s[20:23], m0 offen sc1 lds\n"
"buffer_load_dwordx4 v255, s[20:23], m0 offen offset:1024 sc1 lds\n"
"v_mul_f32 v195, v195, v201\n"
"v_cvt_pk_bf16_f32 v211, v194, v195\n"
"ds_write_b128 v249, v[208:211]\n"
"v_exp_f32 v200, v200\n"
"v_mul_f32 v201, s56, v190\n"
"s_waitcnt lgkmcnt(4)\n"
"v_mfma_f32_16x16x32_bf16 v[168:171], v[152:155], v[80:83], v[168:171]\n"
"ds_read_b64_tr_b16 v[146:147], v253 offset:512\n"
"s_add_u32 m0, s57, 73728\n"
"v_mfma_f32_16x16x32_bf16 v[172:175], v[152:155], v[84:87], v[172:175]\n"
"ds_read_b64_tr_b16 v[152:153], v253 offset:1024\n"
"buffer_load_dwordx4 v255, s[20:23], m0 offen sc1 lds\n"
"buffer_load_dwordx4 v255, s[20:23], m0 offen offset:1024 sc1 lds\n"
"v_rcp_f32 v202, v202\n"
"v_add_f32 v200, 1.0, v200\n"
"v_mul_f32 v188, v188, v202\n"
"v_exp_f32 v201, v201\n"
"v_mul_f32 v202, s56, v191\n"
"v_rcp_f32 v200, v200\n"
"s_waitcnt lgkmcnt(4)\n"
"v_mfma_f32_16x16x32_bf16 v[168:171], v[160:163], v[88:91], v[168:171]\n"
"ds_read_b64_tr_b16 v[154:155], v253 offset:1536\n"
"v_mfma_f32_16x16x32_bf16 v[172:175], v[160:163], v[92:95], v[172:175]\n"
"ds_read_b64_tr_b16 v[160:161], v253 offset:2048\n"
"ds_read_b64_tr_b16 v[162:163], v253 offset:2560\n"
"buffer_load_dwordx4 v255, s[20:23], m0 offen offset:2048 sc1 lds\n"
"buffer_load_dwordx4 v255, s[20:23], m0 offen offset:3072 sc1 lds\n"
"v_add_f32 v201, 1.0, v201\n"
"v_mul_f32 v189, v189, v200\n"
"v_cvt_pk_bf16_f32 v212, v188, v189\n"
"v_exp_f32 v202, v202\n"
"v_mul_f32 v200, s56, v196\n"
"v_rcp_f32 v201, v201\n"
"s_waitcnt lgkmcnt(4)\n"
"v_mfma_f32_16x16x32_bf16 v[168:171], v[144:147], v[96:99], v[168:171]\n"
"v_mfma_f32_16x16x32_bf16 v[172:175], v[144:147], v[100:103], v[172:175]\n"
"ds_read_b64_tr_b16 v[144:145], v253 offset:3072\n"
"v_add_f32 v202, 1.0, v202\n"
"v_mul_f32 v190, v190, v201\n"
"v_exp_f32 v200, v200\n"
"v_mul_f32 v201, s56, v197\n"
"v_rcp_f32 v202, v202\n"
"v_add_f32 v200, 1.0, v200\n"
"s_waitcnt lgkmcnt(3)\n"
"v_mfma_f32_16x16x32_bf16 v[168:171], v[152:155], v[104:107], v[168:171]\n"
"ds_read_b64_tr_b16 v[146:147], v253 offset:3584\n"
"v_mfma_f32_16x16x32_bf16 v[172:175], v[152:155], v[108:111], v[172:175]\n"
"ds_read_b64_tr_b16 v[152:153], v253 offset:4096\n"
"v_mul_f32 v191, v191, v202\n"
"v_cvt_pk_bf16_f32 v213, v190, v191\n"
"v_exp_f32 v201, v201\n"
"v_mul_f32 v202, s56, v198\n"
"v_rcp_f32 v200, v200\n"
"s_waitcnt lgkmcnt(3)\n"
"v_mfma_f32_16x16x32_bf16 v[168:171], v[160:163], v[112:115], v[168:171]\n"
"ds_read_b64_tr_b16 v[154:155], v253 offset:4608\n"
"v_mfma_f32_16x16x32_bf16 v[172:175], v[160:163], v[116:119], v[172:175]\n"
"ds_read_b64_tr_b16 v[160:161], v253 offset:5120\n"
"ds_read_b64_tr_b16 v[162:163], v253 offset:5632\n"
"v_add_f32 v201, 1.0, v201\n"
"v_mul_f32 v196, v196, v200\n"
"v_exp_f32 v202, v202\n"
"v_mul_f32 v200, s56, v199\n"
"v_rcp_f32 v201, v201\n"
"v_add_f32 v202, 1.0, v202\n"
"s_waitcnt lgkmcnt(4)\n"
"v_mfma_f32_16x16x32_bf16 v[168:171], v[144:147], v[120:123], v[168:171]\n"
"v_mfma_f32_16x16x32_bf16 v[172:175], v[144:147], v[124:127], v[172:175]\n"
"s_add_u32 m0, m0, 4096\n"
"s_barrier\n"
"buffer_load_dwordx4 v255, s[20:23], m0 offen sc1 lds\n"
"buffer_load_dwordx4 v255, s[20:23], m0 offen offset:1024 sc1 lds\n"
"v_mul_f32 v197, v197, v201\n"
"v_cvt_pk_bf16_f32 v214, v196, v197\n"
"v_exp_f32 v200, v200\n"
"v_rcp_f32 v202, v202\n"
"v_add_f32 v200, 1.0, v200\n"
"v_mul_f32 v198, v198, v202\n"
"s_waitcnt lgkmcnt(2)\n"
"v_mfma_f32_16x16x32_bf16 v[168:171], v[152:155], v[128:131], v[168:171]\n"
"v_mfma_f32_16x16x32_bf16 v[172:175], v[152:155], v[132:135], v[172:175]\n"
"v_rcp_f32 v200, v200\n"
"s_nop 0\n"
"v_mul_f32 v199, v199, v200\n"
"v_cvt_pk_bf16_f32 v215, v198, v199\n"
"ds_write_b128 v249, v[212:215] offset:1024\n"
"v_add_u32 v249, 0x2000, v249\n"
"s_waitcnt lgkmcnt(1)\n"
"v_mfma_f32_16x16x32_bf16 v[168:171], v[160:163], v[136:139], v[168:171]\n"
"v_mfma_f32_16x16x32_bf16 v[172:175], v[160:163], v[140:143], v[172:175]\n"
"s_waitcnt vmcnt(6)\n"
"s_waitcnt vmcnt(8)\n"
"v_cmp_ne_u64 s[60:61], v[236:237], s[50:51]\n"
"s_nop 1\n"
"s_cmp_lg_u64 s[60:61], 0\n"
"s_cbranch_scc1 O11_%=\n"
"E10_%=:\n"
"s_barrier\n"
"ds_read_b128 v[160:163], v224 offset:0\n"
"ds_read_b128 v[164:167], v226 offset:0\n"
"ds_read_b128 v[104:107], v225 offset:0\n"
"ds_read_b128 v[108:111], v227 offset:0\n"
"s_mov_b32 exec_hi, 0\n"
"v_and_b32 v176, s88, v176\n"
"v_and_b32 v177, s89, v177\n"
"v_and_b32 v178, s90, v178\n"
"s_mov_b32 exec_hi, -1\n"
"v_and_b32 v181, s91, v181\n"
"v_and_b32 v182, s92, v182\n"
"v_and_b32 v183, s93, v183\n"
"s_and_b32 s60, s49, 1\n"
"s_mul_i32 s60, s60, 3072\n"
"s_add_u32 s58, s60, s59\n"
"s_add_u32 m0, s58, 98304\n"
"s_add_u32 s41, s41, s58\n"
"v_mul_f32 v200, s56, v168\n"
"v_mul_f32 v201, s56, v169\n"
"v_mul_f32 v202, s56, v170\n"
"v_mul_f32 v203, s56, v171\n"
"v_mul_f32 v204, s56, v172\n"
"v_mul_f32 v205, s56, v173\n"
"v_mul_f32 v206, s56, v174\n"
"v_mul_f32 v207, s56, v175\n"
"s_waitcnt lgkmcnt(2)\n"
"v_mfma_f32_32x32x16_bf16 v[184:199], v[160:163], v[176:179], 0\n"
"ds_read_b128 v[112:115], v224 offset:19456\n"
"ds_read_b128 v[116:119], v226 offset:19456\n"
"v_exp_f32 v200, v200\n"
"v_exp_f32 v201, v201\n"
"v_exp_f32 v202, v202\n"
"v_exp_f32 v203, v203\n"
"v_mfma_f32_32x32x16_bf16 v[184:199], v[164:167], v[180:183], v[184:199]\n"
"ds_read_b64_tr_b16 v[136:137], v230 offset:0\n"
"ds_read_b64_tr_b16 v[138:139], v231 offset:0\n"
"ds_read_b64_tr_b16 v[140:141], v230 offset:256\n"
"ds_read_b64_tr_b16 v[142:143], v231 offset:256\n"
"v_exp_f32 v204, v204\n"
"v_exp_f32 v205, v205\n"
"v_exp_f32 v206, v206\n"
"v_exp_f32 v207, v207\n"
"buffer_load_dwordx4 v255, s[28:31], s41 offen sc1 lds\n"
"s_and_b32 s60, s49, 1\n"
"s_mul_i32 s60, s60, 3072\n"
"v_add_f32 v200, 1.0, v200\n"
"v_add_f32 v201, 1.0, v201\n"
"v_add_f32 v202, 1.0, v202\n"
"v_add_f32 v203, 1.0, v203\n"
"v_add_f32 v204, 1.0, v204\n"
"v_add_f32 v205, 1.0, v205\n"
"v_add_f32 v206, 1.0, v206\n"
"v_add_f32 v207, 1.0, v207\n"
"v_max3_f32 v240, v184, v185, v186\n"
"v_rcp_f32 v200, v200\n"
"v_max3_f32 v240, v187, v188, v240\n"
"v_rcp_f32 v201, v201\n"
"v_max3_f32 v240, v189, v190, v240\n"
"v_rcp_f32 v202, v202\n"
"v_max3_f32 v240, v191, v192, v240\n"
"v_rcp_f32 v203, v203\n"
"v_max3_f32 v240, v193, v194, v240\n"
"v_rcp_f32 v204, v204\n"
"v_max3_f32 v240, v195, v196, v240\n"
"v_rcp_f32 v205, v205\n"
"v_max3_f32 v240, v197, v198, v240\n"
"v_rcp_f32 v206, v206\n"
"v_max_f32 v240, v199, v240\n"
"v_rcp_f32 v207, v207\n"
"s_mov_b32 exec_lo, 0\n"
"v_cvt_pk_bf16_f32 v180, -v240, 0\n"
"s_mov_b32 exec_lo, -1\n"
"s_waitcnt lgkmcnt(6)\n"
"v_mfma_f32_32x32x16_bf16 v[144:159], v[104:107], v[176:179], 0\n"
"buffer_load_dwordx4 v255, s[28:31], s41 offen offset:1024 sc1 lds\n"
"s_add_u32 m0, m0, 8192\n"
"s_add_u32 s41, s41, 8192\n"
"v_subrev_u32 v249, s60, v249\n"
"v_mul_f32 v200, v200, v168\n"
"v_mul_f32 v201, v201, v169\n"
"v_mul_f32 v202, v202, v170\n"
"v_mul_f32 v203, v203, v171\n"
"v_mfma_f32_32x32x16_bf16 v[144:159], v[108:111], v[180:183], v[144:159]\n"
"v_mul_f32 v204, v204, v172\n"
"v_mul_f32 v205, v205, v173\n"
"v_mul_f32 v206, v206, v174\n"
"v_mul_f32 v207, v207, v175\n"
"v_cvt_pk_bf16_f32 v208, v200, v201\n"
"v_cvt_pk_bf16_f32 v209, v202, v203\n"
"v_cvt_pk_bf16_f32 v210, v204, v205\n"
"v_cvt_pk_bf16_f32 v211, v206, v207\n"
"ds_write_b128 v249, v[208:211]\n"
"v_add_u32 v253, 122880, v255\n"
"v_add_u32 v254, 98304, v255\n"
"v_add_u32 v241, 155648, v255\n"
"ds_read_b128 v[48:51], v253 offset:24576\n"
"ds_read_b128 v[52:55], v253 offset:25600\n"
"v_exp_f32 v144, v144\n"
"v_exp_f32 v145, v145\n"
"ds_read_b64_tr_b16 v[104:105], v228 offset:19456\n"
"ds_read_b64_tr_b16 v[106:107], v229 offset:19456\n"
"ds_read_b64_tr_b16 v[108:109], v228 offset:19712\n"
"ds_read_b64_tr_b16 v[110:111], v229 offset:19712\n"
"buffer_load_dwordx4 v255, s[28:31], s41 offen sc1 lds\n"
"v_exp_f32 v146, v146\n"
"v_exp_f32 v147, v147\n"
"v_cvt_pk_bf16_f32 v144, v144, v145\n"
"v_cvt_pk_bf16_f32 v145, v146, v147\n"
"s_waitcnt lgkmcnt(11)\n"
"v_mfma_f32_32x32x16_bf16 v[184:199], v[112:115], v[176:179], 0\n"
"ds_read_b128 v[120:123], v225 offset:19456\n"
"ds_read_b128 v[124:127], v227 offset:19456\n"
"ds_read_b128 v[56:59], v253 offset:28672\n"
"ds_read_b128 v[60:63], v253 offset:29696\n"
"buffer_load_dwordx4 v255, s[28:31], s41 offen offset:1024 sc1 lds\n"
"s_add_u32 m0, m0, 8192\n"
"s_add_u32 s41, s41, 8192\n"
"v_exp_f32 v148, v148\n"
"v_exp_f32 v149, v149\n"
"v_exp_f32 v150, v150\n"
"v_exp_f32 v151, v151\n"
"v_cvt_pk_bf16_f32 v146, v148, v149\n"
"v_cvt_pk_bf16_f32 v147, v150, v151\n"
"v_mfma_f32_32x32x16_bf16 v[184:199], v[116:119], v[180:183], v[184:199]\n"
"ds_read_b64_tr_b16 v[168:169], v228 offset:0\n"
"ds_read_b64_tr_b16 v[170:171], v229 offset:0\n"
"ds_read_b64_tr_b16 v[172:173], v228 offset:256\n"
"ds_read_b64_tr_b16 v[174:175], v229 offset:256\n"
"v_exp_f32 v152, v152\n"
"v_exp_f32 v153, v153\n"
"v_exp_f32 v154, v154\n"
"v_exp_f32 v155, v155\n"
"s_waitcnt lgkmcnt(15)\n"
"v_mfma_f32_32x32x16_bf16 v[200:215], v[136:139], v[144:147], 0\n"
"v_cvt_pk_bf16_f32 v148, v152, v153\n"
"v_cvt_pk_bf16_f32 v149, v154, v155\n"
"ds_read_b128 v[16:19], v253 offset:8192\n"
"ds_read_b128 v[20:23], v253 offset:9216\n"
"v_exp_f32 v156, v156\n"
"v_exp_f32 v157, v157\n"
"v_exp_f32 v158, v158\n"
"v_exp_f32 v159, v159\n"
"v_cvt_pk_bf16_f32 v150, v156, v157\n"
"v_cvt_pk_bf16_f32 v151, v158, v159\n"
"v_exp_f32 v184, v184\n"
"v_exp_f32 v185, v185\n"
"v_mfma_f32_32x32x16_bf16 v[200:215], v[140:143], v[148:151], v[200:215]\n"
"ds_read_b128 v[128:131], v224 offset:38912\n"
"ds_read_b128 v[132:135], v226 offset:38912\n"
"ds_read_b128 v[24:27], v253 offset:12288\n"
"ds_read_b128 v[28:31], v253 offset:13312\n"
"s_mul_i32 s60, s48, 663552\n"
"s_add_u32 s60, s60, 3072\n"
"s_mul_i32 s61, s34, 589824\n"
"s_add_u32 s62, s60, 589824\n"
"v_exp_f32 v186, v186\n"
"v_exp_f32 v187, v187\n"
"v_cvt_pk_bf16_f32 v184, v184, v185\n"
"v_cvt_pk_bf16_f32 v185, v186, v187\n"
"s_waitcnt lgkmcnt(12)\n"
"v_mfma_f32_32x32x16_bf16 v[144:159], v[120:123], v[176:179], 0\n"
"ds_read_b64_tr_b16 v[112:113], v230 offset:19456\n"
"ds_read_b64_tr_b16 v[114:115], v231 offset:19456\n"
"ds_read_b64_tr_b16 v[116:117], v230 offset:19712\n"
"ds_read_b64_tr_b16 v[118:119], v231 offset:19712\n"
"s_sub_u32 s62, s62, s61\n"
"s_add_u32 s61, s61, s60\n"
"s_mul_i32 s63, s40, 576\n"
"s_add_u32 s68, s61, 0x0\n"
"v_exp_f32 v188, v188\n"
"v_exp_f32 v189, v189\n"
"v_exp_f32 v190, v190\n"
"v_exp_f32 v191, v191\n"
"v_cvt_pk_bf16_f32 v186, v188, v189\n"
"v_cvt_pk_bf16_f32 v187, v190, v191\n"
"v_mfma_f32_32x32x16_bf16 v[144:159], v[124:127], v[180:183], v[144:159]\n"
"ds_read_b64_tr_b16 v[80:81], v228 offset:38912\n"
"ds_read_b64_tr_b16 v[82:83], v229 offset:38912\n"
"ds_read_b64_tr_b16 v[84:85], v228 offset:39168\n"
"ds_read_b64_tr_b16 v[86:87], v229 offset:39168\n"
"s_add_u32 s77, s62, 0xfffff800\n"
"s_add_u32 s69, s61, 0x8c00\n"
"s_add_u32 s78, s62, 0x8400\n"
"s_add_u32 s61, s61, s63\n"
"v_exp_f32 v192, v192\n"
"v_exp_f32 v193, v193\n"
"v_exp_f32 v194, v194\n"
"v_exp_f32 v195, v195\n"
"s_waitcnt lgkmcnt(15)\n"
"v_mfma_f32_32x32x16_bf16 v[200:215], v[104:107], v[184:187], v[200:215]\n"
"v_cvt_pk_bf16_f32 v188, v192, v193\n"
"v_cvt_pk_bf16_f32 v189, v194, v195\n"
"ds_read_b128 v[0:3], v253 offset:0\n"
"ds_read_b128 v[4:7], v253 offset:1024\n"
"ds_read_b128 v[8:11], v253 offset:4096\n"
"ds_read_b128 v[12:15], v253 offset:5120\n"
"s_sub_u32 s62, s62, s63\n"
"s_add_u32 s70, s61, 0xfffff800\n"
"s_add_u32 s79, s62, 0x0\n"
"s_add_u32 s71, s61, 0x8400\n"
"v_exp_f32 v196, v196\n"
"v_exp_f32 v197, v197\n"
"v_exp_f32 v198, v198\n"
"v_exp_f32 v199, v199\n"
"v_cvt_pk_bf16_f32 v190, v196, v197\n"
"v_cvt_pk_bf16_f32 v191, v198, v199\n"
"s_waitcnt vmcnt(4) lgkmcnt(15)\n"
"s_barrier\n"
"v_exp_f32 v144, v144\n"
"v_exp_f32 v145, v145\n"
"v_mfma_f32_32x32x16_bf16 v[200:215], v[108:111], v[188:191], v[200:215]\n"
"ds_read_b128 v[72:75], v225 offset:38912\n"
"ds_read_b128 v[76:79], v227 offset:38912\n"
"ds_read_b128 v[120:123], v224 offset:58368\n"
"ds_read_b128 v[124:127], v226 offset:58368\n"
"s_add_u32 s80, s62, 0x8c00\n"
"s_add_u32 s61, s61, s63\n"
"s_sub_u32 s62, s62, s63\n"
"s_add_u32 s72, s61, 0x0\n"
"v_exp_f32 v146, v146\n"
"v_exp_f32 v147, v147\n"
"v_cvt_pk_bf16_f32 v144, v144, v145\n"
"v_cvt_pk_bf16_f32 v145, v146, v147\n"
"s_waitcnt lgkmcnt(15)\n"
"v_mfma_f32_32x32x16_bf16 v[184:199], v[128:131], v[176:179], 0\n"
"ds_read_b64_tr_b16 v[88:89], v230 offset:38912\n"
"ds_read_b64_tr_b16 v[90:91], v231 offset:38912\n"
"ds_read_b64_tr_b16 v[92:93], v230 offset:39168\n"
"ds_read_b64_tr_b16 v[94:95], v231 offset:39168\n"
"s_add_u32 s81, s62, 0xfffff800\n"
"s_add_u32 s73, s61, 0x8c00\n"
"s_add_u32 s82, s62, 0x8400\n"
"s_add_u32 s61, s61, s63\n"
"v_exp_f32 v148, v148\n"
"v_exp_f32 v149, v149\n"
"v_exp_f32 v150, v150\n"
"v_exp_f32 v151, v151\n"
"v_cvt_pk_bf16_f32 v146, v148, v149\n"
"v_cvt_pk_bf16_f32 v147, v150, v151\n"
"v_mfma_f32_32x32x16_bf16 v[184:199], v[132:135], v[180:183], v[184:199]\n"
"ds_read_b64_tr_b16 v[96:97], v228 offset:58368\n"
"ds_read_b64_tr_b16 v[98:99], v229 offset:58368\n"
"ds_read_b64_tr_b16 v[100:101], v228 offset:58624\n"
"ds_read_b64_tr_b16 v[102:103], v229 offset:58624\n"
"v_add_u32 v228, 19456, v228\n"
"v_add_u32 v229, 19456, v229\n"
"s_sub_u32 s62, s62, s63\n"
"s_add_u32 s74, s61, 0xfffff800\n"
"s_add_u32 s83, s62, 0x0\n"
"s_add_u32 s75, s61, 0x9000\n"
"v_exp_f32 v152, v152\n"
"v_exp_f32 v153, v153\n"
"v_exp_f32 v154, v154\n"
"v_exp_f32 v155, v155\n"
"s_waitcnt lgkmcnt(15)\n"
"v_mfma_f32_32x32x16_bf16 v[200:215], v[112:115], v[144:147], v[200:215]\n"
"v_cvt_pk_bf16_f32 v148, v152, v153\n"
"v_cvt_pk_bf16_f32 v149, v154, v155\n"
"ds_read_b64_tr_b16 v[136:137], v228 offset:58368\n"
"ds_read_b64_tr_b16 v[138:139], v229 offset:58368\n"
"ds_read_b64_tr_b16 v[140:141], v228 offset:58624\n"
"ds_read_b64_tr_b16 v[142:143], v229 offset:58624\n"
"ds_read_b128 v[32:35], v253 offset:16384\n"
"ds_read_b128 v[36:39], v253 offset:17408\n"
"s_add_u32 s84, s62, 0x8c00\n"
"s_lshr_b32 s61, s49, 1\n"
"s_add_u32 s62, s61, s34\n"
"s_sub_u32 s61, s61, s34\n"
"v_exp_f32 v156, v156\n"
"v_exp_f32 v157, v157\n"
"v_exp_f32 v158, v158\n"
"v_exp_f32 v159, v159\n"
"v_cvt_pk_bf16_f32 v150, v156, v157\n"
"v_cvt_pk_bf16_f32 v151, v158, v159\n"
"v_exp_f32 v184, v184\n"
"v_exp_f32 v185, v185\n"
"v_mfma_f32_32x32x16_bf16 v[200:215], v[116:119], v[148:151], v[200:215]\n"
"v_add_u32 v224, 19456, v224\n"
"v_add_u32 v226, 19456, v226\n"
"ds_read_b64_tr_b16 v[104:105], v230 offset:58368\n"
"ds_read_b64_tr_b16 v[106:107], v231 offset:58368\n"
"ds_read_b64_tr_b16 v[108:109], v230 offset:58624\n"
"ds_read_b64_tr_b16 v[110:111], v231 offset:58624\n"
"s_mul_i32 s62, s62, 18432\n"
"s_mul_i32 s61, s61, 18432\n"
"s_add_u32 s62, s62, s60\n"
"s_add_u32 s61, s61, s60\n"
"v_exp_f32 v186, v186\n"
"v_exp_f32 v187, v187\n"
"v_cvt_pk_bf16_f32 v184, v184, v185\n"
"v_cvt_pk_bf16_f32 v185, v186, v187\n"
"s_waitcnt lgkmcnt(15)\n"
"v_mfma_f32_32x32x16_bf16 v[144:159], v[72:75], v[176:179], 0\n"
"ds_read_b128 v[128:131], v225 offset:58368\n"
"ds_read_b128 v[132:135], v227 offset:58368\n"
"ds_read_b128 v[112:115], v224 offset:58368\n"
"ds_read_b128 v[116:119], v226 offset:58368\n"
"s_add_u32 s76, s62, 293888\n"
"s_add_u32 s85, s61, 311296\n"
"s_mul_i32 s86, s48, 82944\n"
"s_add_u32 s87, s86, 40448\n"
"v_exp_f32 v188, v188\n"
"v_exp_f32 v189, v189\n"
"v_exp_f32 v190, v190\n"
"v_exp_f32 v191, v191\n"
"v_cvt_pk_bf16_f32 v186, v188, v189\n"
"v_cvt_pk_bf16_f32 v187, v190, v191\n"
"v_mfma_f32_32x32x16_bf16 v[144:159], v[76:79], v[180:183], v[144:159]\n"
"ds_read_b128 v[40:43], v253 offset:20480\n"
"ds_read_b128 v[44:47], v253 offset:21504\n"
"ds_read2_b64 v[64:67], v241 offset0:0 offset1:128\n"
"ds_read2_b64 v[68:71], v241 offset0:1 offset1:129\n"
"s_mul_i32 s57, s49, 7168\n"
"s_mov_b64 s[36:37], s[8:9]\n"
"s_mov_b32 s38, 663552\n"
"v_add_u32 v250, 1024, v235\n"
"v_exp_f32 v192, v192\n"
"v_exp_f32 v193, v193\n"
"v_exp_f32 v194, v194\n"
"v_exp_f32 v195, v195\n"
"s_waitcnt lgkmcnt(15)\n"
"v_mfma_f32_32x32x16_bf16 v[200:215], v[80:83], v[184:187], v[200:215]\n"
"v_cvt_pk_bf16_f32 v188, v192, v193\n"
"v_cvt_pk_bf16_f32 v189, v194, v195\n"
"v_add_u32 v249, 1024, v244\n"
"s_sub_u32 s24, s12, 3072\n"
"s_subb_u32 s25, s13, 0\n"
"s_mov_b32 s26, 5311488\n"
"v_exp_f32 v196, v196\n"
"v_exp_f32 v197, v197\n"
"v_exp_f32 v198, v198\n"
"v_exp_f32 v199, v199\n"
"v_cvt_pk_bf16_f32 v190, v196, v197\n"
"v_cvt_pk_bf16_f32 v191, v198, v199\n"
"s_waitcnt vmcnt(0) lgkmcnt(0)\n"
"s_barrier\n"
"v_exp_f32 v144, v144\n"
"v_exp_f32 v145, v145\n"
"v_mfma_f32_32x32x16_bf16 v[200:215], v[84:87], v[188:191], v[200:215]\n"
"ds_read_b128 v[72:75], v254 offset:0\n"
"ds_read_b128 v[76:79], v254 offset:1024\n"
"s_add_u32 m0, s57, 0\n"
"s_nop 0\n"
"buffer_load_dwordx4 v250, s[24:27], s68 offen lds\n"
"buffer_load_dwordx4 v250, s[24:27], s69 offen offset:1024 lds\n"
"v_exp_f32 v146, v146\n"
"v_exp_f32 v147, v147\n"
"v_cvt_pk_bf16_f32 v144, v144, v145\n"
"v_cvt_pk_bf16_f32 v145, v146, v147\n"
"s_waitcnt lgkmcnt(15)\n"
"v_mfma_f32_32x32x16_bf16 v[184:199], v[120:123], v[176:179], 0\n"
"ds_read_b128 v[80:83], v254 offset:4096\n"
"ds_read_b128 v[84:87], v254 offset:5120\n"
"buffer_load_dwordx4 v250, s[24:27], s70 offen offset:2048 lds\n"
"buffer_load_dwordx4 v250, s[24:27], s71 offen offset:3072 lds\n"
"v_exp_f32 v148, v148\n"
"v_exp_f32 v149, v149\n"
"v_exp_f32 v150, v150\n"
"v_exp_f32 v151, v151\n"
"v_cvt_pk_bf16_f32 v146, v148, v149\n"
"v_cvt_pk_bf16_f32 v147, v150, v151\n"
"v_mfma_f32_32x32x16_bf16 v[184:199], v[124:127], v[180:183], v[184:199]\n"
"s_add_u32 m0, m0, 4096\n"
"s_nop 0\n"
"buffer_load_dwordx4 v250, s[24:27], s72 offen lds\n"
"buffer_load_dwordx4 v250, s[24:27], s73 offen offset:1024 lds\n"
"v_exp_f32 v152, v152\n"
"v_exp_f32 v153, v153\n"
"v_exp_f32 v154, v154\n"
"v_exp_f32 v155, v155\n"
"s_waitcnt lgkmcnt(15)\n"
"v_mfma_f32_32x32x16_bf16 v[200:215], v[88:91], v[144:147], v[200:215]\n"
"v_cvt_pk_bf16_f32 v148, v152, v153\n"
"v_cvt_pk_bf16_f32 v149, v154, v155\n"
"buffer_load_dwordx4 v250, s[24:27], s74 offen offset:2048 lds\n"
"v_exp_f32 v156, v156\n"
"v_exp_f32 v157, v157\n"
"v_exp_f32 v158, v158\n"
"v_exp_f32 v159, v159\n"
"v_cvt_pk_bf16_f32 v150, v156, v157\n"
"v_cvt_pk_bf16_f32 v151, v158, v159\n"
"v_exp_f32 v184, v184\n"
"v_exp_f32 v185, v185\n"
"v_mfma_f32_32x32x16_bf16 v[200:215], v[92:95], v[148:151], v[200:215]\n"
"s_add_u32 m0, s57, 32768\n"
"s_nop 0\n"
"buffer_load_dwordx4 v250, s[24:27], s75 offen lds\n"
"buffer_load_dwordx4 v250, s[24:27], s76 offen offset:1024 lds\n"
"v_exp_f32 v186, v186\n"
"v_exp_f32 v187, v187\n"
"v_cvt_pk_bf16_f32 v184, v184, v185\n"
"v_cvt_pk_bf16_f32 v185, v186, v187\n"
"s_waitcnt lgkmcnt(10)\n"
"v_mfma_f32_32x32x16_bf16 v[144:159], v[128:131], v[176:179], 0\n"
"ds_read_b128 v[88:91], v254 offset:8192\n"
"ds_read_b128 v[92:95], v254 offset:9216\n"
"buffer_load_dwordx4 v250, s[24:27], s77 offen offset:2048 lds\n"
"buffer_load_dwordx4 v250, s[24:27], s78 offen offset:3072 lds\n"
"v_exp_f32 v188, v188\n"
"v_exp_f32 v189, v189\n"
"v_exp_f32 v190, v190\n"
"v_exp_f32 v191, v191\n"
"v_cvt_pk_bf16_f32 v186, v188, v189\n"
"v_cvt_pk_bf16_f32 v187, v190, v191\n"
"v_mfma_f32_32x32x16_bf16 v[144:159], v[132:135], v[180:183], v[144:159]\n"
"s_add_u32 m0, m0, 4096\n"
"s_nop 0\n"
"buffer_load_dwordx4 v250, s[24:27], s79 offen lds\n"
"buffer_load_dwordx4 v250, s[24:27], s80 offen offset:1024 lds\n"
"v_exp_f32 v192, v192\n"
"v_exp_f32 v193, v193\n"
"v_exp_f32 v194, v194\n"
"v_exp_f32 v195, v195\n"
"s_waitcnt lgkmcnt(15)\n"
"v_mfma_f32_32x32x16_bf16 v[200:215], v[96:99], v[184:187], v[200:215]\n"
"v_cvt_pk_bf16_f32 v188, v192, v193\n"
"v_cvt_pk_bf16_f32 v189, v194, v195\n"
"buffer_load_dwordx4 v250, s[24:27], s81 offen offset:2048 lds\n"
"buffer_load_dwordx2 v[236:237], off, s[28:31], s100 offset:8 sc1\n"
"v_exp_f32 v196, v196\n"
"v_exp_f32 v197, v197\n"
"v_exp_f32 v198, v198\n"
"v_exp_f32 v199, v199\n"
"v_cvt_pk_bf16_f32 v190, v196, v197\n"
"v_cvt_pk_bf16_f32 v191, v198, v199\n"
"v_exp_f32 v144, v144\n"
"v_exp_f32 v145, v145\n"
"v_mfma_f32_32x32x16_bf16 v[200:215], v[100:103], v[188:191], v[200:215]\n"
"s_add_u32 m0, s57, 65536\n"
"s_nop 0\n"
"buffer_load_dwordx4 v249, s[36:39], s86 offen lds\n"
"buffer_load_dwordx4 v249, s[36:39], s87 offen offset:1024 lds\n"
"v_exp_f32 v146, v146\n"
"v_exp_f32 v147, v147\n"
"v_cvt_pk_bf16_f32 v144, v144, v145\n"
"v_cvt_pk_bf16_f32 v145, v146, v147\n"
"s_waitcnt lgkmcnt(15)\n"
"v_mfma_f32_32x32x16_bf16 v[184:199], v[160:163], v[176:179], 0\n"
"ds_read_b128 v[96:99], v254 offset:12288\n"
"ds_read_b128 v[100:103], v254 offset:13312\n"
"buffer_load_dwordx4 v250, s[24:27], s82 offen offset:3072 lds\n"
"v_exp_f32 v148, v148\n"
"v_exp_f32 v149, v149\n"
"v_exp_f32 v150, v150\n"
"v_exp_f32 v151, v151\n"
"s_waitcnt lgkmcnt(12)\n"
"v_mfma_f32_32x32x16_bf16 v[120:135], v[112:115], v[176:179], 0\n"
"v_cvt_pk_bf16_f32 v146, v148, v149\n"
"v_cvt_pk_bf16_f32 v147, v150, v151\n"
"v_mfma_f32_32x32x16_bf16 v[184:199], v[164:167], v[180:183], v[184:199]\n"
"s_add_u32 m0, m0, 4096\n"
"s_nop 0\n"
"buffer_load_dwordx4 v250, s[24:27], s83 offen lds\n"
"buffer_load_dwordx4 v250, s[24:27], s84 offen offset:1024 lds\n"
"v_exp_f32 v152, v152\n"
"v_exp_f32 v153, v153\n"
"v_exp_f32 v154, v154\n"
"v_exp_f32 v155, v155\n"
"s_waitcnt lgkmcnt(15)\n"
"v_mfma_f32_32x32x16_bf16 v[200:215], v[104:107], v[144:147], v[200:215]\n"
"v_cvt_pk_bf16_f32 v148, v152, v153\n"
"v_cvt_pk_bf16_f32 v149, v154, v155\n"
"v_mfma_f32_32x32x16_bf16 v[120:135], v[116:119], v[180:183], v[120:135]\n"
"buffer_load_dwordx4 v250, s[24:27], s85 offen offset:2048 lds\n"
"v_exp_f32 v156, v156\n"
"v_exp_f32 v157, v157\n"
"v_exp_f32 v158, v158\n"
"v_exp_f32 v159, v159\n"
"v_cvt_pk_bf16_f32 v150, v156, v157\n"
"v_cvt_pk_bf16_f32 v151, v158, v159\n"
"s_nop 1\n"
"v_mfma_f32_32x32x16_bf16 v[200:215], v[108:111], v[148:151], v[200:215]\n"
"s_mov_b32 s42, 0x8000\n"
"s_lshl_b32 vcc_lo, -1, 0\n"
"s_lshl_b32 vcc_hi, -1, 4\n"
"v_cndmask_b32 v184, v120, v184, vcc\n"
"s_lshl_b32 vcc_lo, -1, 1\n"
"s_lshl_b32 vcc_hi, -1, 5\n"
"v_cndmask_b32 v185, v121, v185, vcc\n"
"v_exp_f32 v184, v184\n"
"s_lshl_b32 vcc_lo, -1, 2\n"
"s_lshl_b32 vcc_hi, -1, 6\n"
"v_cndmask_b32 v186, v122, v186, vcc\n"
"v_exp_f32 v185, v185\n"
"s_lshl_b32 vcc_lo, -1, 3\n"
"s_lshl_b32 vcc_hi, -1, 7\n"
"v_cndmask_b32 v187, v123, v187, vcc\n"
"v_exp_f32 v186, v186\n"
"s_lshl_b32 vcc_lo, -1, 8\n"
"s_lshl_b32 vcc_hi, -1, 12\n"
"v_cndmask_b32 v188, v124, v188, vcc\n"
"v_exp_f32 v187, v187\n"
"v_cvt_pk_bf16_f32 v184, v184, v185\n"
"v_cvt_pk_bf16_f32 v185, v186, v187\n"
"v_and_b32 v216, v216, v184\n"
"v_and_b32 v217, v217, v185\n"
"v_xor_b32 v184, v216, v184\n"
"v_xor_b32 v185, v217, v185\n"
"s_waitcnt vmcnt(6)\n"
"v_cmp_ne_u64 s[60:61], v[236:237], s[50:51]\n"
"s_nop 1\n"
"s_cmp_lg_u64 s[60:61], 0\n"
"s_cbranch_scc1 O16_%=\n"
"E15_%=:\n"
"s_add_u32 m0, s58, 126976\n"
"s_nop 0\n"
"buffer_load_dwordx4 v255, s[28:31], s41 offen sc1 lds\n"
"buffer_load_dwordx4 v255, s[28:31], s41 offen offset:1024 sc1 lds\n"
"s_add_u32 m0, m0, 8192\n"
"s_add_u32 s41, s41, 8192\n"
"s_waitcnt vmcnt(9) lgkmcnt(0)\n"
"s_barrier\n"
"ds_read_b64_tr_b16 v[164:165], v234 offset:0\n"
"ds_read_b64_tr_b16 v[166:167], v234 offset:14336\n"
"ds_read_b64_tr_b16 v[144:145], v234 offset:1024\n"
"ds_read_b64_tr_b16 v[146:147], v234 offset:15360\n"
"s_lshl_b32 vcc_lo, -1, 9\n"
"s_lshl_b32 vcc_hi, -1, 13\n"
"v_cndmask_b32 v189, v125, v189, vcc\n"
"v_exp_f32 v188, v188\n"
"s_lshl_b32 vcc_lo, -1, 10\n"
"s_lshl_b32 vcc_hi, -1, 14\n"
"v_cndmask_b32 v190, v126, v190, vcc\n"
"v_exp_f32 v189, v189\n"
"s_lshl_b32 vcc_lo, -1, 11\n"
"s_lshl_b32 vcc_hi, -1, 15\n"
"v_cndmask_b32 v191, v127, v191, vcc\n"
"v_exp_f32 v190, v190\n"
"s_lshl_b32 vcc_lo, -1, 16\n"
"s_lshl_b32 vcc_hi, -1, 20\n"
"v_cndmask_b32 v192, v128, v192, vcc\n"
"v_exp_f32 v191, v191\n"
"v_cvt_pk_bf16_f32 v186, v188, v189\n"
"v_cvt_pk_bf16_f32 v187, v190, v191\n"
"v_and_b32 v218, v218, v186\n"
"v_and_b32 v219, v219, v187\n"
"v_xor_b32 v186, v218, v186\n"
"v_xor_b32 v187, v219, v187\n"
"buffer_load_dwordx4 v255, s[28:31], s41 offen sc1 lds\n"
"buffer_load_dwordx4 v255, s[28:31], s41 offen offset:1024 sc1 lds\n"
"s_add_u32 m0, s59, 143360\n"
"s_sub_u32 s41, s41, s58\n"
"s_add_u32 s41, s41, s59\n"
"s_add_u32 s41, s41, 8192\n"
"ds_read_b64_tr_b16 v[148:149], v234 offset:2048\n"
"ds_read_b64_tr_b16 v[150:151], v234 offset:16384\n"
"ds_read_b64_tr_b16 v[152:153], v234 offset:3072\n"
"ds_read_b64_tr_b16 v[154:155], v234 offset:17408\n"
"s_lshl_b32 vcc_lo, -1, 17\n"
"s_lshl_b32 vcc_hi, -1, 21\n"
"v_cndmask_b32 v193, v129, v193, vcc\n"
"v_exp_f32 v192, v192\n"
"s_lshl_b32 vcc_lo, -1, 18\n"
"s_lshl_b32 vcc_hi, -1, 22\n"
"v_cndmask_b32 v194, v130, v194, vcc\n"
"v_exp_f32 v193, v193\n"
"s_lshl_b32 vcc_lo, -1, 19\n"
"s_lshl_b32 vcc_hi, -1, 23\n"
"v_cndmask_b32 v195, v131, v195, vcc\n"
"v_exp_f32 v194, v194\n"
"s_lshl_b32 vcc_lo, -1, 24\n"
"s_lshl_b32 vcc_hi, -1, 28\n"
"v_cndmask_b32 v196, v132, v196, vcc\n"
"v_exp_f32 v195, v195\n"
"s_waitcnt lgkmcnt(15)\n"
"v_mfma_f32_32x32x16_bf16 v[200:215], v[168:171], v[216:219], v[200:215]\n"
"v_cvt_pk_bf16_f32 v188, v192, v193\n"
"v_cvt_pk_bf16_f32 v189, v194, v195\n"
"v_and_b32 v220, v220, v188\n"
"v_and_b32 v221, v221, v189\n"
"v_xor_b32 v188, v220, v188\n"
"v_xor_b32 v189, v221, v189\n"
"v_mfma_f32_32x32x16_bf16 v[200:215], v[136:139], v[184:187], v[200:215]\n"
"buffer_load_dwordx4 v255, s[28:31], s41 offen sc1 lds\n"
"ds_read_b64_tr_b16 v[156:157], v234 offset:4096\n"
"ds_read_b64_tr_b16 v[158:159], v234 offset:18432\n"
"ds_read_b64_tr_b16 v[160:161], v234 offset:5120\n"
"ds_read_b64_tr_b16 v[162:163], v234 offset:19456\n"
"s_lshl_b32 vcc_lo, -1, 25\n"
"s_lshl_b32 vcc_hi, -1, 29\n"
"v_cndmask_b32 v197, v133, v197, vcc\n"
"v_exp_f32 v196, v196\n"
"s_lshl_b32 vcc_lo, -1, 26\n"
"s_lshl_b32 vcc_hi, -1, 30\n"
"v_cndmask_b32 v198, v134, v198, vcc\n"
"v_exp_f32 v197, v197\n"
"s_lshl_b32 vcc_lo, -1, 27\n"
"s_lshl_b32 vcc_hi, -1, 31\n"
"v_cndmask_b32 v199, v135, v199, vcc\n"
"v_exp_f32 v198, v198\n"
"v_exp_f32 v199, v199\n"
"v_cvt_pk_bf16_f32 v190, v196, v197\n"
"v_cvt_pk_bf16_f32 v191, v198, v199\n"
"v_and_b32 v222, v222, v190\n"
"v_and_b32 v223, v223, v191\n"
"v_xor_b32 v190, v222, v190\n"
"v_xor_b32 v191, v223, v191\n"
"v_mfma_f32_32x32x16_bf16 v[200:215], v[172:175], v[220:223], v[200:215]\n"
"s_nop 0\n"
"v_mfma_f32_32x32x16_bf16 v[200:215], v[140:143], v[188:191], v[200:215]\n"
"v_and_b32 v228, 0x3ff, v234\n"
"v_add_u32 v228, 147456, v228\n"
"v_add_u32 v230, 126976, v255\n"
"v_add_u32 v241, 16384, v230\n"
"v_add_u32 v242, 28672, v255\n"
"v_lshrrev_b32 v243, 1, v255\n"
"v_add_u32 v243, 94208, v243\n"
"s_mov_b32 s43, 8\n"
"s_mov_b32 s45, 1\n"
"s_mul_i32 s55, s47, 589824\n"
"s_lshl_b32 s60, s48, 13\n"
"s_add_u32 s55, s55, s60\n"
"s_lshl_b32 s60, s46, 10\n"
"s_add_u32 s55, s55, s60\n"
"s_add_u32 s55, s55, 16842752\n"
"v_subrev_u32 v240, 0x2b800000, v212\n"
"v_cmp_lt_u32 vcc, 0x28000000, v240\n"
"s_and_b32 s62, s95, 1\n"
"s_sub_u32 s62, 0, s62\n"
"s_mov_b32 s63, 0\n"
"s_cbranch_vccnz LFB_%=\n"
"LFRD_%=:\n"
"v_rcp_f32 v240, v212\n"
"v_cndmask_b32 v200, v200, v210, s[62:63]\n"
"v_cndmask_b32 v201, v201, v211, s[62:63]\n"
"v_mul_f32 v240, v240, v245\n"
"ds_read_b64_tr_b16 v[188:189], v234 offset:6144\n"
"ds_read_b64_tr_b16 v[190:191], v234 offset:20480\n"
"v_add_u32 v253, s42, v234\n"
"s_add_u32 m0, s57, 0x18000\n"
"s_add_u32 s63, s48, 1\n"
"s_and_b32 s63, s63, 7\n"
"s_lshl_b32 s61, s63, 7\n"
"v_add_u32 v250, s61, v235\n"
"v_add_u32 v249, s61, v244\n"
"s_waitcnt lgkmcnt(10)\n"
"v_mfma_f32_16x16x32_bf16 v[192:195], v[164:167], v[0:3], 0\n"
"v_mfma_f32_16x16x32_bf16 v[196:199], v[164:167], v[4:7], 0\n"
"ds_read_b64_tr_b16 v[164:165], v253 offset:0\n"
"buffer_load_dwordx4 v250, s[24:27], s68 offen lds\n"
"v_mul_f32 v200, v200, v240\n"
"v_mul_f32 v201, v201, v240\n"
"v_mul_f32 v202, v202, v240\n"
"v_mul_f32 v203, v203, v240\n"
"v_mul_f32 v204, v204, v240\n"
"v_mfma_f32_16x16x32_bf16 v[192:195], v[144:147], v[8:11], v[192:195]\n"
"ds_read_b64_tr_b16 v[166:167], v253 offset:14336\n"
"v_mfma_f32_16x16x32_bf16 v[196:199], v[144:147], v[12:15], v[196:199]\n"
"ds_read_b64_tr_b16 v[168:169], v253 offset:1024\n"
"buffer_load_dwordx4 v250, s[24:27], s69 offen offset:1024 lds\n"
"s_add_u32 s42, s42, 0x8000\n"
"s_and_b32 s42, s42, 0x1ffff\n"
"v_add_u32 v254, s42, v234\n"
"v_mul_f32 v205, v205, v240\n"
"v_mul_f32 v206, v206, v240\n"
"v_mul_f32 v207, v207, v240\n"
"v_mul_f32 v208, v208, v240\n"
"v_mul_f32 v209, v209, v240\n"
"s_waitcnt lgkmcnt(9)\n"
"v_mfma_f32_16x16x32_bf16 v[192:195], v[148:151], v[16:19], v[192:195]\n"
"ds_read_b64_tr_b16 v[170:171], v253 offset:15360\n"
"v_mfma_f32_16x16x32_bf16 v[196:199], v[148:151], v[20:23], v[196:199]\n"
"ds_read_b64_tr_b16 v[172:173], v253 offset:2048\n"
"buffer_load_dwordx4 v250, s[24:27], s70 offen offset:2048 lds\n"
"v_cvt_pk_bf16_f32 v216, v200, v201\n"
"v_cvt_pk_bf16_f32 v217, v202, v203\n"
"v_cvt_pk_bf16_f32 v218, v204, v205\n"
"v_cvt_pk_bf16_f32 v219, v206, v207\n"
"v_mfma_f32_16x16x32_bf16 v[192:195], v[152:155], v[24:27], v[192:195]\n"
"ds_read_b64_tr_b16 v[174:175], v253 offset:16384\n"
"v_mfma_f32_16x16x32_bf16 v[196:199], v[152:155], v[28:31], v[196:199]\n"
"ds_read_b64_tr_b16 v[176:177], v253 offset:3072\n"
"buffer_load_dwordx4 v250, s[24:27], s71 offen offset:3072 lds\n"
"v_cvt_pk_bf16_f32 v220, v208, v209\n"
"ds_write_b128 v232, v[216:219]\n"
"s_mov_b32 exec_lo, 0\n"
"v_mov_b32 v220, 0\n"
"s_mov_b32 exec_lo, -1\n"
"ds_write_b32 v233, v220\n"
"s_waitcnt lgkmcnt(11)\n"
"v_mfma_f32_16x16x32_bf16 v[192:195], v[156:159], v[32:35], v[192:195]\n"
"ds_read_b64_tr_b16 v[178:179], v253 offset:17408\n"
"v_mfma_f32_16x16x32_bf16 v[196:199], v[156:159], v[36:39], v[196:199]\n"
"ds_read_b64_tr_b16 v[180:181], v253 offset:4096\n"
"s_add_u32 m0, s57, 102400\n"
"s_nop 0\n"
"buffer_load_dwordx4 v250, s[24:27], s72 offen lds\n"
"s_mov_b32 s52, s53\n"
"s_mov_b32 s53, s43\n"
"v_add_u32 v229, 12288, v228\n"
"s_add_u32 s43, s48, s45\n"
"s_and_b32 s43, s43, 7\n"
"v_mfma_f32_16x16x32_bf16 v[192:195], v[160:163], v[40:43], v[192:195]\n"
"ds_read_b64_tr_b16 v[182:183], v253 offset:18432\n"
"v_mfma_f32_16x16x32_bf16 v[196:199], v[160:163], v[44:47], v[196:199]\n"
"ds_read_b64_tr_b16 v[184:185], v253 offset:5120\n"
"buffer_load_dwordx4 v250, s[24:27], s73 offen offset:1024 lds\n"
"s_waitcnt lgkmcnt(11)\n"
"v_mfma_f32_16x16x32_bf16 v[192:195], v[188:191], v[48:51], v[192:195]\n"
"ds_read_b64_tr_b16 v[186:187], v253 offset:19456\n"
"v_mfma_f32_16x16x32_bf16 v[196:199], v[188:191], v[52:55], v[196:199]\n"
"ds_read_b64_tr_b16 v[188:189], v253 offset:6144\n"
"ds_read_b64_tr_b16 v[190:191], v253 offset:20480\n"
"buffer_load_dwordx4 v250, s[24:27], s74 offen offset:2048 lds\n"
"v_mfma_f32_16x16x32_bf16 v[192:195], v[164:167], v[56:59], v[192:195]\n"
"v_mfma_f32_16x16x32_bf16 v[196:199], v[164:167], v[60:63], v[196:199]\n"
"s_waitcnt vmcnt(7) lgkmcnt(4)\n"
"s_barrier\n"
"ds_read_b64_tr_b16 v[164:165], v254 offset:0\n"
"s_add_u32 m0, s57, 0\n"
"s_nop 0\n"
"buffer_load_dwordx4 v250, s[24:27], s75 offen lds\n"
"ds_read_b128 v[104:107], v230 offset:0\n"
"ds_read_b128 v[108:111], v230 offset:1024\n"
"ds_read_b128 v[144:147], v242 offset:0\n"
"ds_read_b128 v[148:151], v242 offset:1024\n"
"s_waitcnt lgkmcnt(15)\n"
"v_mfma_f32_16x16x32_bf16 v[192:195], v[168:171], v[64:67], v[192:195]\n"
"ds_read_b64_tr_b16 v[166:167], v254 offset:14336\n"
"v_mfma_f32_16x16x32_bf16 v[196:199], v[168:171], v[68:71], v[196:199]\n"
"ds_read_b64_tr_b16 v[168:169], v254 offset:1024\n"
"buffer_load_dwordx4 v250, s[24:27], s76 offen offset:1024 lds\n"
"s_add_u32 s42, s42, 0x8000\n"
"s_and_b32 s42, s42, 0x1ffff\n"
"v_add_u32 v252, s42, v234\n"
"ds_read_b128 v[152:155], v242 offset:32768\n"
"ds_read_b128 v[156:159], v242 offset:33792\n"
"v_mfma_f32_16x16x32_bf16 v[192:195], v[172:175], v[72:75], v[192:195]\n"
"ds_read_b64_tr_b16 v[170:171], v254 offset:15360\n"
"v_mfma_f32_16x16x32_bf16 v[196:199], v[172:175], v[76:79], v[196:199]\n"
"ds_read_b64_tr_b16 v[172:173], v229\n"
"buffer_load_dwordx4 v250, s[24:27], s77 offen offset:2048 lds\n"
"buffer_load_dwordx4 v250, s[24:27], s78 offen offset:3072 lds\n"
"ds_read_b64 v[160:161], v243 offset:0\n"
"ds_read_b64 v[162:163], v243 offset:512\n"
"s_waitcnt lgkmcnt(15)\n"
"v_mfma_f32_16x16x32_bf16 v[192:195], v[176:179], v[80:83], v[192:195]\n"
"v_mfma_f32_16x16x32_bf16 v[196:199], v[176:179], v[84:87], v[196:199]\n"
"ds_read_b64_tr_b16 v[176:177], v254 offset:3072\n"
"s_add_u32 m0, s57, 4096\n"
"s_nop 0\n"
"buffer_load_dwordx4 v250, s[24:27], s79 offen lds\n"
"ds_read_b128 v[112:115], v230 offset:4096\n"
"ds_read_b128 v[116:119], v230 offset:5120\n"
"v_mfma_f32_16x16x32_bf16 v[192:195], v[180:183], v[88:91], v[192:195]\n"
"ds_read_b64_tr_b16 v[178:179], v254 offset:17408\n"
"v_mfma_f32_16x16x32_bf16 v[196:199], v[180:183], v[92:95], v[196:199]\n"
"ds_read_b64_tr_b16 v[180:181], v254 offset:4096\n"
"buffer_load_dwordx4 v250, s[24:27], s80 offen offset:1024 lds\n"
"buffer_load_dwordx4 v250, s[24:27], s81 offen offset:2048 lds\n"
"ds_read_b128 v[120:123], v230 offset:8192\n"
"ds_read_b128 v[124:127], v230 offset:9216\n"
"s_waitcnt lgkmcnt(15)\n"
"v_mfma_f32_16x16x32_bf16 v[192:195], v[184:187], v[96:99], v[192:195]\n"
"ds_read_b64_tr_b16 v[182:183], v254 offset:18432\n"
"v_mfma_f32_16x16x32_bf16 v[196:199], v[184:187], v[100:103], v[196:199]\n"
"ds_read_b64_tr_b16 v[184:185], v254 offset:5120\n"
"s_add_u32 m0, s57, 32768\n"
"s_nop 0\n"
"buffer_load_dwordx4 v249, s[36:39], s86 offen lds\n"
"ds_read_b128 v[128:131], v230 offset:12288\n"
"ds_read_b128 v[132:135], v230 offset:13312\n"
"v_mfma_f32_16x16x32_bf16 v[192:195], v[188:191], v[104:107], v[192:195]\n"
"ds_read_b64_tr_b16 v[186:187], v254 offset:19456\n"
"v_mfma_f32_16x16x32_bf16 v[196:199], v[188:191], v[108:111], v[196:199]\n"
"ds_read_b64_tr_b16 v[188:189], v254 offset:6144\n"
"ds_read_b64_tr_b16 v[190:191], v254 offset:20480\n"
"buffer_load_dwordx4 v249, s[36:39], s87 offen offset:1024 lds\n"
"buffer_load_dwordx4 v250, s[24:27], s82 offen offset:3072 lds\n"
"ds_read2_b64 v[136:139], v241 offset0:0 offset1:128\n"
"ds_read2_b64 v[140:143], v241 offset0:1 offset1:129\n"
"s_waitcnt lgkmcnt(15)\n"
"v_mfma_f32_16x16x32_bf16 v[200:203], v[164:167], v[144:147], 0\n"
"v_mfma_f32_16x16x32_bf16 v[204:207], v[164:167], v[148:151], 0\n"
"s_waitcnt vmcnt(10) lgkmcnt(8)\n"
"s_barrier\n"
"ds_read_b64_tr_b16 v[164:165], v252 offset:0\n"
"s_add_u32 m0, s57, 36864\n"
"s_nop 0\n"
"buffer_load_dwordx4 v250, s[24:27], s83 offen lds\n"
"v_mfma_f32_16x16x32_bf16 v[200:203], v[168:171], v[152:155], v[200:203]\n"
"ds_read_b64_tr_b16 v[166:167], v252 offset:14336\n"
"v_mfma_f32_16x16x32_bf16 v[204:207], v[168:171], v[156:159], v[204:207]\n"
"ds_read_b64_tr_b16 v[168:169], v252 offset:1024\n"
"buffer_load_dwordx4 v250, s[24:27], s84 offen offset:1024 lds\n"
"buffer_load_dwordx4 v250, s[24:27], s85 offen offset:2048 lds\n"
"s_add_u32 s42, s42, 0x8000\n"
"s_and_b32 s42, s42, 0x1ffff\n"
"v_add_u32 v253, s42, v234\n"
"s_xor_b32 s60, s42, 0x10000\n"
"s_add_u32 m0, s60, s57\n"
"s_add_u32 s63, s43, 1\n"
"s_and_b32 s63, s63, 7\n"
"s_lshl_b32 s61, s63, 7\n"
"v_add_u32 v250, s61, v235\n"
"v_add_u32 v249, s61, v244\n"
"s_waitcnt lgkmcnt(15)\n"
"v_mfma_f32_16x16x16_bf16 v[200:203], v[172:173], v[160:161], v[200:203]\n"
"ds_read_b64_tr_b16 v[170:171], v252 offset:15360\n"
"v_mfma_f32_16x16x16_bf16 v[204:207], v[172:173], v[162:163], v[204:207]\n"
"ds_read_b64_tr_b16 v[172:173], v252 offset:2048\n"
"buffer_load_dwordx4 v250, s[24:27], s68 offen lds\n"
"v_mfma_f32_16x16x32_bf16 v[192:195], v[176:179], v[112:115], v[192:195]\n"
"ds_read_b64_tr_b16 v[174:175], v252 offset:16384\n"
"v_mfma_f32_16x16x32_bf16 v[196:199], v[176:179], v[116:119], v[196:199]\n"
"ds_read_b64_tr_b16 v[176:177], v252 offset:3072\n"
"buffer_load_dwordx4 v250, s[24:27], s69 offen offset:1024 lds\n"
"v_bfe_u32 v240, v255, 4, 4\n"
"v_lshrrev_b32 v241, 8, v255\n"
"v_lshlrev_b32 v241, 2, v241\n"
"v_sub_u32 v248, v240, v241\n"
"v_cmp_gt_u32 vcc, 4, v248\n"
"v_mov_b32 v240, 0x3f80\n"
"v_mov_b32 v241, 0\n"
"s_waitcnt lgkmcnt(11)\n"
"v_mfma_f32_16x16x32_bf16 v[192:195], v[180:183], v[120:123], v[192:195]\n"
"ds_read_b64_tr_b16 v[178:179], v252 offset:17408\n"
"v_mfma_f32_16x16x32_bf16 v[196:199], v[180:183], v[124:127], v[196:199]\n"
"ds_read_b64_tr_b16 v[180:181], v252 offset:4096\n"
"buffer_load_dwordx4 v250, s[24:27], s70 offen offset:2048 lds\n"
"v_cndmask_b32 v240, 0, v240, vcc\n"
"v_lshlrev_b32 v248, 4, v248\n"
"v_lshlrev_b64 v[240:241], v248, v[240:241]\n"
"s_mov_b64 s[20:21], s[16:17]\n"
"s_mov_b32 s22, 2359296\n"
"s_mov_b32 s23, 0x20000\n"
"v_mfma_f32_16x16x32_bf16 v[192:195], v[184:187], v[128:131], v[192:195]\n"
"ds_read_b64_tr_b16 v[182:183], v252 offset:18432\n"
"v_mfma_f32_16x16x32_bf16 v[196:199], v[184:187], v[132:135], v[196:199]\n"
"ds_read_b64_tr_b16 v[184:185], v252 offset:5120\n"
"buffer_load_dwordx4 v250, s[24:27], s71 offen offset:3072 lds\n"
"s_add_u32 m0, m0, 4096\n"
"v_and_b32 v251, 0x70, v255\n"
"v_lshrrev_b32 v248, 7, v255\n"
"v_lshl_add_u32 v248, v248, 13, v251\n"
"s_sub_u32 s62, 8, s48\n"
"v_and_b32 v245, 0x80, v255\n"
"v_mad_u32_u24 v251, v245, s62, v251\n"
"v_lshrrev_b32 v251, 1, v251\n"
"s_lshr_b32 s60, 0xf0f0, s62\n"
"s_or_b32 s40, s60, 0xff00\n"
"s_lshr_b32 s60, 0x0c0c, s62\n"
"s_and_b32 s66, s60, 0xff\n"
"s_cmp_eq_u32 s46, 4\n"
"s_cselect_b32 s67, 8, 0\n"
"s_waitcnt lgkmcnt(11)\n"
"v_mfma_f32_16x16x32_bf16 v[208:211], v[188:191], v[136:139], v[192:195]\n"
"ds_read_b64_tr_b16 v[186:187], v252 offset:19456\n"
"v_mfma_f32_16x16x32_bf16 v[212:215], v[188:191], v[140:143], v[196:199]\n"
"ds_read_b64_tr_b16 v[188:189], v252 offset:6144\n"
"ds_read_b64_tr_b16 v[190:191], v252 offset:20480\n"
"buffer_load_dwordx4 v250, s[24:27], s72 offen lds\n"
"s_mul_i32 s60, s47, 9\n"
"s_add_u32 s60, s60, s48\n"
"s_lshl_b32 s60, s60, 6\n"
"s_add_u32 s35, s60, 12288\n"
"s_lshl_b32 s41, s48, 13\n"
"s_sub_u32 s60, s55, s41\n"
"s_lshl_b32 s61, s48, 16\n"
"s_add_u32 s44, s60, s61\n"
"s_mul_i32 s61, s46, 896\n"
"s_sub_u32 s60, s60, s61\n"
"s_lshl_b32 s61, s48, 10\n"
"s_add_u32 s60, s60, s61\n"
"s_add_u32 s56, s60, 524288\n"
"s_sub_u32 s41, s41, 8192\n"
"s_and_b32 s41, s41, 0xe000\n"
"s_add_u32 s33, s44, s41\n"
"s_sub_u32 s41, s41, 8192\n"
"s_and_b32 s41, s41, 0xe000\n"
"s_add_u32 s34, s44, s41\n"
"s_mov_b32 s45, 2\n"
"LG2D_%=:\n"
"s_waitcnt lgkmcnt(10)\n"
"v_mfma_f32_16x16x32_bf16 v[192:195], v[164:167], v[0:3], 0\n"
"v_mfma_f32_16x16x32_bf16 v[196:199], v[164:167], v[4:7], 0\n"
"s_lshl_b32 s62, s53, 16\n"
"s_add_u32 s62, s62, s55\n"
"v_fma_f32 v208, v208, v246, v200\n"
"v_fma_f32 v209, v209, v246, v201\n"
"v_fma_f32 v210, v210, v246, v202\n"
"v_fma_f32 v211, v211, v246, v203\n"
"v_fma_f32 v212, v212, v247, v204\n"
"v_cvt_pk_bf16_f32 v216, v208, v209\n"
"v_cvt_pk_bf16_f32 v217, v210, v211\n"
"v_fma_f32 v213, v213, v247, v205\n"
"v_fma_f32 v214, v214, v247, v206\n"
"v_fma_f32 v215, v215, v247, v207\n"
"v_cvt_pk_bf16_f32 v218, v212, v213\n"
"v_cvt_pk_bf16_f32 v219, v214, v215\n"
"buffer_store_dwordx4 v[216:219], v255, s[28:31], s62 offen sc1\n"
"s_waitcnt vmcnt(12) lgkmcnt(4)\n"
"s_barrier\n"
"ds_read_b64_tr_b16 v[164:165], v253 offset:0\n"
"buffer_load_dwordx4 v250, s[24:27], s73 offen offset:1024 lds\n"
"v_mfma_f32_16x16x32_bf16 v[192:195], v[168:171], v[8:11], v[192:195]\n"
"ds_read_b64_tr_b16 v[166:167], v253 offset:14336\n"
"v_mfma_f32_16x16x32_bf16 v[196:199], v[168:171], v[12:15], v[196:199]\n"
"ds_read_b64_tr_b16 v[168:169], v253 offset:1024\n"
"buffer_load_dwordx4 v250, s[24:27], s74 offen offset:2048 lds\n"
"s_add_u32 s42, s42, 0x8000\n"
"s_and_b32 s42, s42, 0x1ffff\n"
"v_add_u32 v254, s42, v234\n"
"s_xor_b32 s60, s42, 0x10000\n"
"s_add_u32 m0, s60, s57\n"
"s_waitcnt lgkmcnt(9)\n"
"v_mfma_f32_16x16x32_bf16 v[192:195], v[172:175], v[16:19], v[192:195]\n"
"ds_read_b64_tr_b16 v[170:171], v253 offset:15360\n"
"v_mfma_f32_16x16x32_bf16 v[196:199], v[172:175], v[20:23], v[196:199]\n"
"ds_read_b64_tr_b16 v[172:173], v253 offset:2048\n"
"buffer_load_dwordx4 v250, s[24:27], s75 offen lds\n"
"v_mfma_f32_16x16x32_bf16 v[192:195], v[176:179], v[24:27], v[192:195]\n"
"ds_read_b64_tr_b16 v[174:175], v253 offset:16384\n"
"v_mfma_f32_16x16x32_bf16 v[196:199], v[176:179], v[28:31], v[196:199]\n"
"ds_read_b64_tr_b16 v[176:177], v253 offset:3072\n"
"buffer_load_dwordx4 v250, s[24:27], s76 offen offset:1024 lds\n"
"s_waitcnt lgkmcnt(9)\n"
"v_mfma_f32_16x16x32_bf16 v[192:195], v[180:183], v[32:35], v[192:195]\n"
"ds_read_b64_tr_b16 v[178:179], v253 offset:17408\n"
"v_mfma_f32_16x16x32_bf16 v[196:199], v[180:183], v[36:39], v[196:199]\n"
"ds_read_b64_tr_b16 v[180:181], v253 offset:4096\n"
"buffer_load_dwordx4 v250, s[24:27], s77 offen offset:2048 lds\n"
"s_mov_b32 s52, s53\n"
"s_mov_b32 s53, s43\n"
"s_lshl_b32 s60, s53, 10\n"
"v_add_u32 v229, s60, v228\n"
"s_add_u32 s43, s48, s45\n"
"s_and_b32 s43, s43, 7\n"
"v_mfma_f32_16x16x32_bf16 v[192:195], v[184:187], v[40:43], v[192:195]\n"
"ds_read_b64_tr_b16 v[182:183], v253 offset:18432\n"
"v_mfma_f32_16x16x32_bf16 v[196:199], v[184:187], v[44:47], v[196:199]\n"
"ds_read_b64_tr_b16 v[184:185], v253 offset:5120\n"
"buffer_load_dwordx4 v250, s[24:27], s78 offen offset:3072 lds\n"
"s_add_u32 m0, m0, 4096\n"
"s_waitcnt lgkmcnt(9)\n"
"v_mfma_f32_16x16x32_bf16 v[192:195], v[188:191], v[48:51], v[192:195]\n"
"ds_read_b64_tr_b16 v[186:187], v253 offset:19456\n"
"v_mfma_f32_16x16x32_bf16 v[196:199], v[188:191], v[52:55], v[196:199]\n"
"ds_read_b64_tr_b16 v[188:189], v253 offset:6144\n"
"ds_read_b64_tr_b16 v[190:191], v253 offset:20480\n"
"buffer_load_dwordx4 v250, s[24:27], s79 offen lds\n"
"v_mfma_f32_16x16x32_bf16 v[192:195], v[164:167], v[56:59], v[192:195]\n"
"v_mfma_f32_16x16x32_bf16 v[196:199], v[164:167], v[60:63], v[196:199]\n"
"s_waitcnt vmcnt(13) lgkmcnt(4)\n"
"s_barrier\n"
"ds_read_b64_tr_b16 v[164:165], v254 offset:0\n"
"buffer_load_dwordx4 v250, s[24:27], s80 offen offset:1024 lds\n"
"s_waitcnt lgkmcnt(9)\n"
"v_mfma_f32_16x16x32_bf16 v[192:195], v[168:171], v[64:67], v[192:195]\n"
"ds_read_b64_tr_b16 v[166:167], v254 offset:14336\n"
"v_mfma_f32_16x16x32_bf16 v[196:199], v[168:171], v[68:71], v[196:199]\n"
"ds_read_b64_tr_b16 v[168:169], v254 offset:1024\n"
"buffer_load_dwordx4 v250, s[24:27], s81 offen offset:2048 lds\n"
"s_add_u32 s42, s42, 0x8000\n"
"s_and_b32 s42, s42, 0x1ffff\n"
"v_add_u32 v252, s42, v234\n"
"s_xor_b32 s60, s42, 0x10000\n"
"s_add_u32 m0, s60, s57\n"
"v_mfma_f32_16x16x32_bf16 v[192:195], v[172:175], v[72:75], v[192:195]\n"
"ds_read_b64_tr_b16 v[170:171], v254 offset:15360\n"
"v_mfma_f32_16x16x32_bf16 v[196:199], v[172:175], v[76:79], v[196:199]\n"
"ds_read_b64_tr_b16 v[172:173], v229\n"
"buffer_load_dwordx4 v249, s[36:39], s86 offen lds\n"
"s_waitcnt lgkmcnt(9)\n"
"v_mfma_f32_16x16x32_bf16 v[192:195], v[176:179], v[80:83], v[192:195]\n"
"v_mfma_f32_16x16x32_bf16 v[196:199], v[176:179], v[84:87], v[196:199]\n"
"ds_read_b64_tr_b16 v[176:177], v254 offset:3072\n"
"buffer_load_dwordx4 v249, s[36:39], s87 offen offset:1024 lds\n"
"v_mfma_f32_16x16x32_bf16 v[192:195], v[180:183], v[88:91], v[192:195]\n"
"ds_read_b64_tr_b16 v[178:179], v254 offset:17408\n"
"v_mfma_f32_16x16x32_bf16 v[196:199], v[180:183], v[92:95], v[196:199]\n"
"ds_read_b64_tr_b16 v[180:181], v254 offset:4096\n"
"s_waitcnt lgkmcnt(8)\n"
"v_mfma_f32_16x16x32_bf16 v[192:195], v[184:187], v[96:99], v[192:195]\n"
"ds_read_b64_tr_b16 v[182:183], v254 offset:18432\n"
"v_mfma_f32_16x16x32_bf16 v[196:199], v[184:187], v[100:103], v[196:199]\n"
"ds_read_b64_tr_b16 v[184:185], v254 offset:5120\n"
"buffer_load_dwordx4 v250, s[24:27], s82 offen offset:3072 lds\n"
"s_add_u32 m0, m0, 4096\n"
"v_mfma_f32_16x16x32_bf16 v[192:195], v[188:191], v[104:107], v[192:195]\n"
"ds_read_b64_tr_b16 v[186:187], v254 offset:19456\n"
"v_mfma_f32_16x16x32_bf16 v[196:199], v[188:191], v[108:111], v[196:199]\n"
"ds_read_b64_tr_b16 v[188:189], v254 offset:6144\n"
"ds_read_b64_tr_b16 v[190:191], v254 offset:20480\n"
"buffer_load_dwordx4 v250, s[24:27], s83 offen lds\n"
"s_waitcnt lgkmcnt(9)\n"
"v_mfma_f32_16x16x32_bf16 v[200:203], v[164:167], v[144:147], 0\n"
"v_mfma_f32_16x16x32_bf16 v[204:207], v[164:167], v[148:151], 0\n"
"s_waitcnt vmcnt(11) lgkmcnt(4)\n"
"s_barrier\n"
"ds_read_b64_tr_b16 v[164:165], v252 offset:0\n"
"buffer_load_dwordx4 v250, s[24:27], s84 offen offset:1024 lds\n"
"v_mfma_f32_16x16x32_bf16 v[200:203], v[168:171], v[152:155], v[200:203]\n"
"ds_read_b64_tr_b16 v[166:167], v252 offset:14336\n"
"v_mfma_f32_16x16x32_bf16 v[204:207], v[168:171], v[156:159], v[204:207]\n"
"ds_read_b64_tr_b16 v[168:169], v252 offset:1024\n"
"buffer_load_dwordx4 v250, s[24:27], s85 offen offset:2048 lds\n"
"s_add_u32 s42, s42, 0x8000\n"
"s_and_b32 s42, s42, 0x1ffff\n"
"v_add_u32 v253, s42, v234\n"
"s_cmp_eq_u32 s45, 8\n"
"s_cbranch_scc1 LG3_%=\n"
"s_xor_b32 s60, s42, 0x10000\n"
"s_add_u32 m0, s60, s57\n"
"s_add_u32 s63, s43, 1\n"
"s_and_b32 s63, s63, 7\n"
"s_lshl_b32 s61, s63, 7\n"
"v_add_u32 v250, s61, v235\n"
"v_add_u32 v249, s61, v244\n"
"s_waitcnt lgkmcnt(9)\n"
"v_mfma_f32_16x16x16_bf16 v[200:203], v[172:173], v[160:161], v[200:203]\n"
"ds_read_b64_tr_b16 v[170:171], v252 offset:15360\n"
"v_mfma_f32_16x16x16_bf16 v[204:207], v[172:173], v[162:163], v[204:207]\n"
"ds_read_b64_tr_b16 v[172:173], v252 offset:2048\n"
"buffer_load_dwordx4 v250, s[24:27], s68 offen lds\n"
"v_mfma_f32_16x16x32_bf16 v[192:195], v[176:179], v[112:115], v[192:195]\n"
"ds_read_b64_tr_b16 v[174:175], v252 offset:16384\n"
"v_mfma_f32_16x16x32_bf16 v[196:199], v[176:179], v[116:119], v[196:199]\n"
"ds_read_b64_tr_b16 v[176:177], v252 offset:3072\n"
"buffer_load_dwordx4 v250, s[24:27], s69 offen offset:1024 lds\n"
"s_waitcnt lgkmcnt(9)\n"
"v_mfma_f32_16x16x32_bf16 v[192:195], v[180:183], v[120:123], v[192:195]\n"
"ds_read_b64_tr_b16 v[178:179], v252 offset:17408\n"
"v_mfma_f32_16x16x32_bf16 v[196:199], v[180:183], v[124:127], v[196:199]\n"
"ds_read_b64_tr_b16 v[180:181], v252 offset:4096\n"
"buffer_load_dwordx4 v250, s[24:27], s70 offen offset:2048 lds\n"
"v_mfma_f32_16x16x32_bf16 v[192:195], v[184:187], v[128:131], v[192:195]\n"
"ds_read_b64_tr_b16 v[182:183], v252 offset:18432\n"
"v_mfma_f32_16x16x32_bf16 v[196:199], v[184:187], v[132:135], v[196:199]\n"
"ds_read_b64_tr_b16 v[184:185], v252 offset:5120\n"
"buffer_load_dwordx4 v250, s[24:27], s71 offen offset:3072 lds\n"
"s_add_u32 m0, m0, 4096\n"
"s_waitcnt lgkmcnt(11)\n"
"v_mfma_f32_16x16x32_bf16 v[208:211], v[188:191], v[136:139], v[192:195]\n"
"ds_read_b64_tr_b16 v[186:187], v252 offset:19456\n"
"v_mfma_f32_16x16x32_bf16 v[212:215], v[188:191], v[140:143], v[196:199]\n"
"ds_read_b64_tr_b16 v[188:189], v252 offset:6144\n"
"ds_read_b64_tr_b16 v[190:191], v252 offset:20480\n"
"buffer_load_dwordx4 v250, s[24:27], s72 offen lds\n"
"s_add_u32 s45, s45, 1\n"
"s_branch LG2D_%=\n"
"LG3_%=:\n"
"buffer_load_dwordx4 v[230:233], v248, s[28:31], s56 offen sc1\n"
"buffer_load_dwordx4 v[220:223], v255, s[28:31], s33 offen sc1\n"
"buffer_load_dwordx4 v[224:227], v255, s[28:31], s34 offen sc1\n"
"s_waitcnt lgkmcnt(9)\n"
"v_mfma_f32_16x16x16_bf16 v[200:203], v[172:173], v[160:161], v[200:203]\n"
"ds_read_b64_tr_b16 v[170:171], v252 offset:15360\n"
"v_mfma_f32_16x16x16_bf16 v[204:207], v[172:173], v[162:163], v[204:207]\n"
"ds_read_b64_tr_b16 v[172:173], v252 offset:2048\n"
"v_mfma_f32_16x16x32_bf16 v[192:195], v[176:179], v[112:115], v[192:195]\n"
"ds_read_b64_tr_b16 v[174:175], v252 offset:16384\n"
"v_mfma_f32_16x16x32_bf16 v[196:199], v[176:179], v[116:119], v[196:199]\n"
"ds_read_b64_tr_b16 v[176:177], v252 offset:3072\n"
"s_waitcnt lgkmcnt(9)\n"
"v_mfma_f32_16x16x32_bf16 v[192:195], v[180:183], v[120:123], v[192:195]\n"
"ds_read_b64_tr_b16 v[178:179], v252 offset:17408\n"
"v_mfma_f32_16x16x32_bf16 v[196:199], v[180:183], v[124:127], v[196:199]\n"
"ds_read_b64_tr_b16 v[180:181], v252 offset:4096\n"
"v_mfma_f32_16x16x32_bf16 v[192:195], v[184:187], v[128:131], v[192:195]\n"
"ds_read_b64_tr_b16 v[182:183], v252 offset:18432\n"
"v_mfma_f32_16x16x32_bf16 v[196:199], v[184:187], v[132:135], v[196:199]\n"
"ds_read_b64_tr_b16 v[184:185], v252 offset:5120\n"
"s_waitcnt lgkmcnt(11)\n"
"v_mfma_f32_16x16x32_bf16 v[208:211], v[188:191], v[136:139], v[192:195]\n"
"ds_read_b64_tr_b16 v[186:187], v252 offset:19456\n"
"v_mfma_f32_16x16x32_bf16 v[212:215], v[188:191], v[140:143], v[196:199]\n"
"ds_read_b64_tr_b16 v[188:189], v252 offset:6144\n"
"ds_read_b64_tr_b16 v[190:191], v252 offset:20480\n"
"v_lshrrev_b32 v244, 4, v255\n"
"s_waitcnt lgkmcnt(10)\n"
"v_mfma_f32_16x16x32_bf16 v[192:195], v[164:167], v[0:3], 0\n"
"v_mfma_f32_16x16x32_bf16 v[196:199], v[164:167], v[4:7], 0\n"
"s_cmp_ge_u32 s46, 4\n"
"s_cbranch_scc1 LG3H_%=\n"
"s_lshl_b32 s62, s53, 16\n"
"s_add_u32 s62, s62, s55\n"
"v_fma_f32 v208, v208, v246, v200\n"
"v_fma_f32 v209, v209, v246, v201\n"
"v_fma_f32 v210, v210, v246, v202\n"
"v_fma_f32 v211, v211, v246, v203\n"
"v_fma_f32 v212, v212, v247, v204\n"
"v_cvt_pk_bf16_f32 v216, v208, v209\n"
"v_cvt_pk_bf16_f32 v217, v210, v211\n"
"v_fma_f32 v213, v213, v247, v205\n"
"v_fma_f32 v214, v214, v247, v206\n"
"v_fma_f32 v215, v215, v247, v207\n"
"v_cvt_pk_bf16_f32 v218, v212, v213\n"
"v_cvt_pk_bf16_f32 v219, v214, v215\n"
"buffer_store_dwordx4 v[216:219], v255, s[28:31], s62 offen sc1\n"
"s_waitcnt vmcnt(10)\n"
"s_barrier\n"
"s_branch LG2X_%=\n"
"LG3H_%=:\n"
"s_barrier\n"
"s_cmp_eq_u32 s46, 4\n"
"s_cbranch_scc0 I39_%=\n"
"s_mov_b64 exec, 0xffff\n"
"buffer_load_dwordx2 v[236:237], v251, s[28:31], s35 offen sc1\n"
"s_mov_b64 exec, -1\n"
"I39_%=:\n"
"s_lshl_b32 s62, s53, 16\n"
"s_add_u32 s62, s62, s55\n"
"v_fma_f32 v208, v208, v246, v200\n"
"v_fma_f32 v209, v209, v246, v201\n"
"v_fma_f32 v210, v210, v246, v202\n"
"v_fma_f32 v211, v211, v246, v203\n"
"v_fma_f32 v212, v212, v247, v204\n"
"v_cvt_pk_bf16_f32 v216, v208, v209\n"
"v_cvt_pk_bf16_f32 v217, v210, v211\n"
"v_fma_f32 v213, v213, v247, v205\n"
"v_fma_f32 v214, v214, v247, v206\n"
"v_fma_f32 v215, v215, v247, v207\n"
"v_cvt_pk_bf16_f32 v218, v212, v213\n"
"v_cvt_pk_bf16_f32 v219, v214, v215\n"
"buffer_store_dwordx4 v[216:219], v255, s[28:31], s62 offen sc1\n"
"LG2X_%=:\n"
"ds_read_b64_tr_b16 v[164:165], v253 offset:0\n"
"v_mfma_f32_16x16x32_bf16 v[192:195], v[168:171], v[8:11], v[192:195]\n"
"ds_read_b64_tr_b16 v[166:167], v253 offset:14336\n"
"v_mfma_f32_16x16x32_bf16 v[196:199], v[168:171], v[12:15], v[196:199]\n"
"ds_read_b64_tr_b16 v[168:169], v253 offset:1024\n"
"s_add_u32 s42, s42, 0x8000\n"
"s_and_b32 s42, s42, 0x1ffff\n"
"v_add_u32 v254, s42, v234\n"
"s_sub_u32 s41, s41, 8192\n"
"s_and_b32 s41, s41, 0xe000\n"
"s_add_u32 s33, s44, s41\n"
"buffer_load_dwordx4 v[0:3], v255, s[28:31], s33 offen sc1\n"
"s_waitcnt lgkmcnt(9)\n"
"v_mfma_f32_16x16x32_bf16 v[192:195], v[172:175], v[16:19], v[192:195]\n"
"ds_read_b64_tr_b16 v[170:171], v253 offset:15360\n"
"v_mfma_f32_16x16x32_bf16 v[196:199], v[172:175], v[20:23], v[196:199]\n"
"ds_read_b64_tr_b16 v[172:173], v253 offset:2048\n"
"v_mfma_f32_16x16x32_bf16 v[192:195], v[176:179], v[24:27], v[192:195]\n"
"ds_read_b64_tr_b16 v[174:175], v253 offset:16384\n"
"v_mfma_f32_16x16x32_bf16 v[196:199], v[176:179], v[28:31], v[196:199]\n"
"ds_read_b64_tr_b16 v[176:177], v253 offset:3072\n"
"s_sub_u32 s41, s41, 8192\n"
"s_and_b32 s41, s41, 0xe000\n"
"s_add_u32 s33, s44, s41\n"
"buffer_load_dwordx4 v[4:7], v255, s[28:31], s33 offen sc1\n"
"s_sub_u32 s41, s41, 8192\n"
"s_and_b32 s41, s41, 0xe000\n"
"s_add_u32 s56, s44, s41\n"
"s_sub_u32 s41, s41, 8192\n"
"s_and_b32 s41, s41, 0xe000\n"
"s_add_u32 s33, s44, s41\n"
"s_sub_u32 s41, s41, 8192\n"
"s_and_b32 s41, s41, 0xe000\n"
"s_add_u32 s34, s44, s41\n"
"s_waitcnt lgkmcnt(9)\n"
"v_mfma_f32_16x16x32_bf16 v[192:195], v[180:183], v[32:35], v[192:195]\n"
"ds_read_b64_tr_b16 v[178:179], v253 offset:17408\n"
"v_mfma_f32_16x16x32_bf16 v[196:199], v[180:183], v[36:39], v[196:199]\n"
"ds_read_b64_tr_b16 v[180:181], v253 offset:4096\n"
"s_mov_b32 s52, s53\n"
"s_mov_b32 s53, s43\n"
"s_lshl_b32 s60, s53, 10\n"
"v_add_u32 v229, s60, v228\n"
"v_mfma_f32_16x16x32_bf16 v[192:195], v[184:187], v[40:43], v[192:195]\n"
"ds_read_b64_tr_b16 v[182:183], v253 offset:18432\n"
"v_mfma_f32_16x16x32_bf16 v[196:199], v[184:187], v[44:47], v[196:199]\n"
"ds_read_b64_tr_b16 v[184:185], v253 offset:5120\n"
"s_waitcnt lgkmcnt(9)\n"
"v_mfma_f32_16x16x32_bf16 v[192:195], v[188:191], v[48:51], v[192:195]\n"
"ds_read_b64_tr_b16 v[186:187], v253 offset:19456\n"
"v_mfma_f32_16x16x32_bf16 v[196:199], v[188:191], v[52:55], v[196:199]\n"
"ds_read_b64_tr_b16 v[188:189], v253 offset:6144\n"
"ds_read_b64_tr_b16 v[190:191], v253 offset:20480\n"
"v_mfma_f32_16x16x32_bf16 v[192:195], v[164:167], v[56:59], v[192:195]\n"
"v_mfma_f32_16x16x32_bf16 v[196:199], v[164:167], v[60:63], v[196:199]\n"
"s_waitcnt vmcnt(6)\n"
"s_barrier\n"
"ds_read_b64_tr_b16 v[164:165], v254 offset:0\n"
"s_waitcnt lgkmcnt(9)\n"
"v_mfma_f32_16x16x32_bf16 v[192:195], v[168:171], v[64:67], v[192:195]\n"
"ds_read_b64_tr_b16 v[166:167], v254 offset:14336\n"
"v_mfma_f32_16x16x32_bf16 v[196:199], v[168:171], v[68:71], v[196:199]\n"
"ds_read_b64_tr_b16 v[168:169], v254 offset:1024\n"
"v_mfma_f32_16x16x32_bf16 v[192:195], v[172:175], v[72:75], v[192:195]\n"
"ds_read_b64_tr_b16 v[170:171], v254 offset:15360\n"
"v_mfma_f32_16x16x32_bf16 v[196:199], v[172:175], v[76:79], v[196:199]\n"
"ds_read_b64_tr_b16 v[172:173], v229\n"
"v_and_b32 v44, 31, v244\n"
"s_lshl_b32 s60, s47, 6\n"
"s_lshr_b32 s61, s46, 2\n"
"s_lshl_b32 s61, s61, 5\n"
"s_add_u32 s61, s61, s60\n"
"v_add_u32 v44, s61, v44\n"
"v_mul_u32_u24 v44, 1152, v44\n"
"v_lshrrev_b32 v40, 5, v244\n"
"v_lshl_add_u32 v44, v40, 4, v44\n"
"s_lshl_b32 s62, s48, 7\n"
"s_lshl_b32 s61, s49, 5\n"
"s_add_u32 s62, s62, s61\n"
"s_waitcnt lgkmcnt(9)\n"
"v_mfma_f32_16x16x32_bf16 v[192:195], v[176:179], v[80:83], v[192:195]\n"
"v_mfma_f32_16x16x32_bf16 v[196:199], v[176:179], v[84:87], v[196:199]\n"
"ds_read_b64_tr_b16 v[176:177], v254 offset:3072\n"
"v_mfma_f32_16x16x32_bf16 v[192:195], v[180:183], v[88:91], v[192:195]\n"
"ds_read_b64_tr_b16 v[178:179], v254 offset:17408\n"
"v_mfma_f32_16x16x32_bf16 v[196:199], v[180:183], v[92:95], v[196:199]\n"
"ds_read_b64_tr_b16 v[180:181], v254 offset:4096\n"
"s_waitcnt lgkmcnt(8)\n"
"v_mfma_f32_16x16x32_bf16 v[192:195], v[184:187], v[96:99], v[192:195]\n"
"ds_read_b64_tr_b16 v[182:183], v254 offset:18432\n"
"v_mfma_f32_16x16x32_bf16 v[196:199], v[184:187], v[100:103], v[196:199]\n"
"ds_read_b64_tr_b16 v[184:185], v254 offset:5120\n"
"v_mfma_f32_16x16x32_bf16 v[192:195], v[188:191], v[104:107], v[192:195]\n"
"ds_read_b64_tr_b16 v[186:187], v254 offset:19456\n"
"v_mfma_f32_16x16x32_bf16 v[196:199], v[188:191], v[108:111], v[196:199]\n"
"ds_read_b64_tr_b16 v[188:189], v254 offset:6144\n"
"ds_read_b64_tr_b16 v[190:191], v254 offset:20480\n"
"s_waitcnt vmcnt(5)\n"
"v_lshlrev_b32 v48, 16, v230\n"
"v_and_b32 v49, 0xffff0000, v230\n"
"v_lshlrev_b32 v50, 16, v231\n"
"v_and_b32 v51, 0xffff0000, v231\n"
"v_lshlrev_b32 v52, 16, v232\n"
"v_and_b32 v53, 0xffff0000, v232\n"
"v_lshlrev_b32 v54, 16, v233\n"
"v_and_b32 v55, 0xffff0000, v233\n"
"s_lshl_b32 s60, s47, 6\n"
"s_lshr_b32 s61, s48, 2\n"
"s_lshl_b32 s61, s61, 5\n"
"s_add_u32 s60, s60, s61\n"
"s_and_b32 s61, s46, 1\n"
"s_lshl3_add_u32 s60, s61, s60\n"
"v_permlane32_swap_b32 v48, v52\n"
"v_permlane32_swap_b32 v49, v53\n"
"v_permlane32_swap_b32 v50, v54\n"
"v_permlane32_swap_b32 v51, v55\n"
"v_add_f32 v48, v48, v52\n"
"v_add_f32 v49, v49, v53\n"
"v_add_f32 v50, v50, v54\n"
"v_add_f32 v51, v51, v55\n"
"v_bfe_u32 v245, v255, 4, 3\n"
"v_lshrrev_b32 v56, 9, v255\n"
"v_permlane16_swap_b32 v48, v50\n"
"v_permlane16_swap_b32 v49, v51\n"
"v_add_f32 v48, v48, v50\n"
"v_add_f32 v49, v49, v51\n"
"v_lshl_add_u32 v245, v56, 4, v245\n"
"v_add_u32 v245, s60, v245\n"
"v_add_f32_dpp v50, v48, v48 row_ror:8 row_mask:0xf bank_mask:0xf\n"
"v_add_f32_dpp v51, v49, v49 row_ror:8 row_mask:0xf bank_mask:0xf\n"
"v_mul_u32_u24 v245, 1152, v245\n"
"v_bfe_u32 v56, v255, 8, 1\n"
"v_lshl_add_u32 v245, v56, 2, v245\n"
"v_cvt_pk_bf16_f32 v230, v50, v51\n"
"s_and_b32 s60, s48, 3\n"
"s_lshl_b32 s60, s60, 5\n"
"s_lshr_b32 s61, s46, 1\n"
"s_lshl3_add_u32 s60, s61, s60\n"
"s_mov_b32 exec_lo, 0x00ff00ff\n"
"s_mov_b32 exec_hi, 0x00ff00ff\n"
"buffer_store_dword v230, v245, s[20:23], s60 offen offset:1024\n"
"s_mov_b64 exec, -1\n"
"s_cmp_eq_u32 s46, 4\n"
"s_cbranch_scc0 I40_%=\n"
"s_waitcnt vmcnt(4)\n"
"v_cmp_ne_u64 s[60:61], v[236:237], s[50:51]\n"
"s_nop 1\n"
"s_and_b32 s60, s60, s66\n"
"s_cbranch_scc1 O45_%=\n"
"E44_%=:\n"
"I40_%=:\n"
"s_barrier\n"
"buffer_load_dwordx4 v[8:11], v255, s[28:31], s56 offen sc1\n"
"buffer_load_dwordx4 v[12:15], v255, s[28:31], s33 offen sc1\n"
"buffer_load_dwordx4 v[16:19], v255, s[28:31], s34 offen sc1\n"
"s_waitcnt lgkmcnt(9)\n"
"v_mfma_f32_16x16x32_bf16 v[200:203], v[164:167], v[144:147], 0\n"
"v_mfma_f32_16x16x32_bf16 v[204:207], v[164:167], v[148:151], 0\n"
"v_mfma_f32_16x16x32_bf16 v[200:203], v[168:171], v[152:155], v[200:203]\n"
"v_mfma_f32_16x16x32_bf16 v[204:207], v[168:171], v[156:159], v[204:207]\n"
"s_waitcnt lgkmcnt(6)\n"
"v_mfma_f32_16x16x16_bf16 v[200:203], v[172:173], v[160:161], v[200:203]\n"
"v_mfma_f32_16x16x16_bf16 v[204:207], v[172:173], v[162:163], v[204:207]\n"
"v_mfma_f32_16x16x32_bf16 v[192:195], v[176:179], v[112:115], v[192:195]\n"
"v_mfma_f32_16x16x32_bf16 v[196:199], v[176:179], v[116:119], v[196:199]\n"
"s_waitcnt lgkmcnt(2)\n"
"v_mfma_f32_16x16x32_bf16 v[192:195], v[180:183], v[120:123], v[192:195]\n"
"v_mfma_f32_16x16x32_bf16 v[196:199], v[180:183], v[124:127], v[196:199]\n"
"v_mfma_f32_16x16x32_bf16 v[192:195], v[184:187], v[128:131], v[192:195]\n"
"v_mfma_f32_16x16x32_bf16 v[196:199], v[184:187], v[132:135], v[196:199]\n"
"s_waitcnt lgkmcnt(0)\n"
"v_mfma_f32_16x16x32_bf16 v[208:211], v[188:191], v[136:139], v[192:195]\n"
"v_mfma_f32_16x16x32_bf16 v[212:215], v[188:191], v[140:143], v[196:199]\n"
"s_nop 6\n"
"v_fma_f32 v208, v208, v246, v200\n"
"v_fma_f32 v209, v209, v246, v201\n"
"v_fma_f32 v210, v210, v246, v202\n"
"v_fma_f32 v211, v211, v246, v203\n"
"v_fma_f32 v212, v212, v247, v204\n"
"v_fma_f32 v213, v213, v247, v205\n"
"v_fma_f32 v214, v214, v247, v206\n"
"v_fma_f32 v215, v215, v247, v207\n"
"s_nop 0\n"
"s_waitcnt vmcnt(7)\n"
"v_mfma_f32_16x16x16_bf16 v[208:211], v[240:241], v[220:221], v[208:211]\n"
"v_mfma_f32_16x16x16_bf16 v[212:215], v[240:241], v[222:223], v[212:215]\n"
"v_mfma_f32_16x16x16_bf16 v[208:211], v[240:241], v[224:225], v[208:211]\n"
"v_mfma_f32_16x16x16_bf16 v[212:215], v[240:241], v[226:227], v[212:215]\n"
"s_waitcnt vmcnt(5)\n"
"v_mfma_f32_16x16x16_bf16 v[208:211], v[240:241], v[0:1], v[208:211]\n"
"v_mfma_f32_16x16x16_bf16 v[212:215], v[240:241], v[2:3], v[212:215]\n"
"s_waitcnt vmcnt(4)\n"
"v_mfma_f32_16x16x16_bf16 v[208:211], v[240:241], v[4:5], v[208:211]\n"
"v_mfma_f32_16x16x16_bf16 v[212:215], v[240:241], v[6:7], v[212:215]\n"
"s_waitcnt vmcnt(2)\n"
"v_mfma_f32_16x16x16_bf16 v[208:211], v[240:241], v[8:9], v[208:211]\n"
"v_mfma_f32_16x16x16_bf16 v[212:215], v[240:241], v[10:11], v[212:215]\n"
"s_waitcnt vmcnt(1)\n"
"v_mfma_f32_16x16x16_bf16 v[208:211], v[240:241], v[12:13], v[208:211]\n"
"v_mfma_f32_16x16x16_bf16 v[212:215], v[240:241], v[14:15], v[212:215]\n"
"s_nop 3\n"
"s_waitcnt vmcnt(0)\n"
"v_mfma_f32_16x16x16_bf16 v[192:195], v[240:241], v[16:17], v[208:211]\n"
"v_mfma_f32_16x16x16_bf16 v[196:199], v[240:241], v[18:19], v[212:215]\n"
"v_max3_u32 v40, v16, v17, v18\n"
"v_max_u32 v40, v40, v19\n"
"v_cmp_eq_u32 s[60:61], -1, v40\n"
"s_nop 1\n"
"s_cmp_lg_u64 s[60:61], 0\n"
"s_cbranch_scc1 O49_%=\n"
"E48_%=:\n"
"s_nop 0\n"
"v_cvt_pk_bf16_f32 v216, v192, v193\n"
"v_cvt_pk_bf16_f32 v217, v194, v195\n"
"v_cvt_pk_bf16_f32 v218, v196, v197\n"
"v_cvt_pk_bf16_f32 v219, v198, v199\n"
"s_nop 1\n"
"v_permlane16_swap_b32 v216, v218\n"
"v_permlane16_swap_b32 v217, v219\n"
"buffer_store_dwordx4 v[216:219], v44, s[20:23], s62 offen\n"
"s_endpgm\n"
"LPH_%=:\n"
"s_waitcnt vmcnt(8)\n"
"s_barrier\n"
"ds_read_b128 v[0:3], v186 offset:0\n"
"ds_read_b64_tr_b16 v[200:201], v253 offset:0\n"
"ds_read_b64_tr_b16 v[202:203], v253 offset:512\n"
"ds_read_b128 v[4:7], v186 offset:2048\n"
"ds_read_b64_tr_b16 v[204:205], v254 offset:0\n"
"ds_read_b64_tr_b16 v[206:207], v254 offset:512\n"
"ds_read_b128 v[8:11], v187 offset:0\n"
"ds_read_b64_tr_b16 v[208:209], v253 offset:1024\n"
"ds_read_b64_tr_b16 v[210:211], v253 offset:1536\n"
"ds_read_b128 v[12:15], v187 offset:2048\n"
"ds_read_b64_tr_b16 v[212:213], v254 offset:1024\n"
"ds_read_b64_tr_b16 v[214:215], v254 offset:1536\n"
"s_add_u32 m0, s57, 139264\n"
"s_nop 0\n"
"buffer_load_dwordx4 v136, s[36:39], s58 offen lds\n"
"buffer_load_dwordx4 v137, s[36:39], s58 offen offset:1024 lds\n"
"buffer_load_dwordx4 v138, s[36:39], s58 offen offset:2048 lds\n"
"buffer_load_dwordx4 v139, s[36:39], s58 offen offset:3072 lds\n"
"s_add_u32 s58, s58, 128\n"
"s_cmp_eq_u32 s58, 1152\n"
"s_cselect_b32 s58, 0, s58\n"
"s_waitcnt lgkmcnt(0)\n"
"s_waitcnt vmcnt(8)\n"
"s_barrier\n"
"ds_read_b128 v[16:19], v184 offset:0\n"
"ds_read_b64_tr_b16 v[144:145], v253 offset:2048\n"
"v_mfma_f32_16x16x32_bf16 v[168:171], v[200:203], v[0:3], 0\n"
"s_add_u32 m0, s57, 122880\n"
"s_nop 0\n"
"buffer_load_dwordx4 v136, s[36:39], s58 offen lds\n"
"s_movk_i32 s60, 1152\n"
"ds_read_b64_tr_b16 v[146:147], v253 offset:2560\n"
"ds_read_b128 v[20:23], v184 offset:2048\n"
"v_mfma_f32_16x16x32_bf16 v[172:175], v[200:203], v[4:7], 0\n"
"buffer_load_dwordx4 v137, s[36:39], s58 offen offset:1024 lds\n"
"ds_read_b64_tr_b16 v[148:149], v254 offset:2048\n"
"ds_read_b64_tr_b16 v[150:151], v254 offset:2560\n"
"v_mfma_f32_16x16x32_bf16 v[176:179], v[204:207], v[0:3], 0\n"
"buffer_load_dwordx4 v138, s[36:39], s58 offen offset:2048 lds\n"
"v_lshrrev_b32 v129, 1, v130\n"
"ds_read_b128 v[24:27], v185 offset:0\n"
"ds_read_b64_tr_b16 v[152:153], v253 offset:3072\n"
"v_mfma_f32_16x16x32_bf16 v[180:183], v[204:207], v[4:7], 0\n"
"buffer_load_dwordx4 v139, s[36:39], s58 offen offset:3072 lds\n"
"s_add_u32 s58, s58, 128\n"
"s_cmp_eq_u32 s58, 1152\n"
"s_cselect_b32 s58, 0, s58\n"
"ds_read_b64_tr_b16 v[154:155], v253 offset:3584\n"
"ds_read_b128 v[28:31], v185 offset:2048\n"
"v_mfma_f32_16x16x32_bf16 v[168:171], v[208:211], v[8:11], v[168:171]\n"
"v_lshl_add_u32 v244, v129, 4, v130\n"
"ds_read_b64_tr_b16 v[156:157], v254 offset:3072\n"
"ds_read_b64_tr_b16 v[158:159], v254 offset:3584\n"
"v_mfma_f32_16x16x32_bf16 v[172:175], v[208:211], v[12:15], v[172:175]\n"
"v_mfma_f32_16x16x32_bf16 v[176:179], v[212:215], v[8:11], v[176:179]\n"
"v_and_b32 v129, 2, v130\n"
"v_mfma_f32_16x16x32_bf16 v[180:183], v[212:215], v[12:15], v[180:183]\n"
"s_waitcnt lgkmcnt(0)\n"
"s_waitcnt vmcnt(8)\n"
"s_barrier\n"
"ds_read_b128 v[32:35], v184 offset:24576\n"
"ds_read_b64_tr_b16 v[200:201], v253 offset:4096\n"
"v_mfma_f32_16x16x32_bf16 v[168:171], v[144:147], v[16:19], v[168:171]\n"
"s_add_u32 m0, s57, 49152\n"
"s_nop 0\n"
"buffer_load_dwordx4 v136, s[36:39], s58 offen lds\n"
"v_sub_u32 v244, v244, v129\n"
"ds_read_b64_tr_b16 v[202:203], v253 offset:4608\n"
"ds_read_b128 v[36:39], v184 offset:26624\n"
"v_mfma_f32_16x16x32_bf16 v[172:175], v[144:147], v[20:23], v[172:175]\n"
"buffer_load_dwordx4 v137, s[36:39], s58 offen offset:1024 lds\n"
"ds_read_b64_tr_b16 v[204:205], v254 offset:4096\n"
"ds_read_b64_tr_b16 v[206:207], v254 offset:4608\n"
"v_mfma_f32_16x16x32_bf16 v[176:179], v[148:151], v[16:19], v[176:179]\n"
"buffer_load_dwordx4 v138, s[36:39], s58 offen offset:2048 lds\n"
"v_add_u32 v244, 16, v244\n"
"ds_read_b128 v[40:43], v185 offset:24576\n"
"ds_read_b64_tr_b16 v[208:209], v253 offset:5120\n"
"v_mfma_f32_16x16x32_bf16 v[180:183], v[148:151], v[20:23], v[180:183]\n"
"buffer_load_dwordx4 v139, s[36:39], s58 offen offset:3072 lds\n"
"s_add_u32 s58, s58, 128\n"
"s_cmp_eq_u32 s58, 1152\n"
"s_cselect_b32 s58, 0, s58\n"
"ds_read_b64_tr_b16 v[210:211], v253 offset:5632\n"
"ds_read_b128 v[44:47], v185 offset:26624\n"
"v_mfma_f32_16x16x32_bf16 v[168:171], v[152:155], v[24:27], v[168:171]\n"
"ds_read_b64_tr_b16 v[212:213], v254 offset:5120\n"
"ds_read_b64_tr_b16 v[214:215], v254 offset:5632\n"
"v_mfma_f32_16x16x32_bf16 v[172:175], v[152:155], v[28:31], v[172:175]\n"
"v_mad_u32_u24 v244, v244, s60, v131\n"
"v_mfma_f32_16x16x32_bf16 v[176:179], v[156:159], v[24:27], v[176:179]\n"
"v_mfma_f32_16x16x32_bf16 v[180:183], v[156:159], v[28:31], v[180:183]\n"
"s_mul_i32 s62, s51, 0x4c957f2d\n"
"s_waitcnt lgkmcnt(0)\n"
"s_waitcnt vmcnt(8)\n"
"s_barrier\n"
"ds_read_b128 v[48:51], v186 offset:16384\n"
"ds_read_b64_tr_b16 v[144:145], v252 offset:0\n"
"v_mfma_f32_16x16x32_bf16 v[168:171], v[200:203], v[32:35], v[168:171]\n"
"s_add_u32 m0, s57, 73728\n"
"s_nop 0\n"
"buffer_load_dwordx4 v136, s[36:39], s58 offen lds\n"
"ds_read_b64_tr_b16 v[146:147], v252 offset:512\n"
"ds_read_b128 v[52:55], v186 offset:18432\n"
"v_mfma_f32_16x16x32_bf16 v[172:175], v[200:203], v[36:39], v[172:175]\n"
"buffer_load_dwordx4 v137, s[36:39], s58 offen offset:1024 lds\n"
"s_mul_hi_u32 s51, s50, 0x4c957f2d\n"
"ds_read_b64_tr_b16 v[148:149], v188 offset:0\n"
"ds_read_b64_tr_b16 v[150:151], v188 offset:512\n"
"v_mfma_f32_16x16x32_bf16 v[176:179], v[204:207], v[32:35], v[176:179]\n"
"buffer_load_dwordx4 v138, s[36:39], s58 offen offset:2048 lds\n"
"ds_read_b128 v[56:59], v187 offset:16384\n"
"ds_read_b64_tr_b16 v[152:153], v252 offset:1024\n"
"v_mfma_f32_16x16x32_bf16 v[180:183], v[204:207], v[36:39], v[180:183]\n"
"buffer_load_dwordx4 v139, s[36:39], s58 offen offset:3072 lds\n"
"s_add_u32 s58, s58, 128\n"
"s_cmp_eq_u32 s58, 1152\n"
"s_cselect_b32 s58, 0, s58\n"
"s_add_u32 s51, s51, s62\n"
"ds_read_b64_tr_b16 v[154:155], v252 offset:1536\n"
"ds_read_b128 v[60:63], v187 offset:18432\n"
"v_mfma_f32_16x16x32_bf16 v[168:171], v[208:211], v[40:43], v[168:171]\n"
"ds_read_b64_tr_b16 v[156:157], v188 offset:1024\n"
"ds_read_b64_tr_b16 v[158:159], v188 offset:1536\n"
"v_mfma_f32_16x16x32_bf16 v[172:175], v[208:211], v[44:47], v[172:175]\n"
"s_mul_i32 s62, s50, 0x5851f42d\n"
"v_mfma_f32_16x16x32_bf16 v[176:179], v[212:215], v[40:43], v[176:179]\n"
"v_mfma_f32_16x16x32_bf16 v[180:183], v[212:215], v[44:47], v[180:183]\n"
"s_waitcnt lgkmcnt(0)\n"
"s_waitcnt vmcnt(8)\n"
"s_barrier\n"
"ds_read_b128 v[64:67], v186 offset:0\n"
"ds_read_b64_tr_b16 v[200:201], v252 offset:2048\n"
"v_mfma_f32_16x16x32_bf16 v[168:171], v[144:147], v[48:51], v[168:171]\n"
"s_add_u32 m0, s57, 139264\n"
"s_nop 0\n"
"buffer_load_dwordx4 v136, s[36:39], s58 offen lds\n"
"s_add_u32 s51, s51, s62\n"
"ds_read_b64_tr_b16 v[202:203], v252 offset:2560\n"
"ds_read_b128 v[68:71], v186 offset:2048\n"
"v_mfma_f32_16x16x32_bf16 v[172:175], v[144:147], v[52:55], v[172:175]\n"
"buffer_load_dwordx4 v137, s[36:39], s58 offen offset:1024 lds\n"
"ds_read_b64_tr_b16 v[204:205], v188 offset:2048\n"
"ds_read_b64_tr_b16 v[206:207], v188 offset:2560\n"
"v_mfma_f32_16x16x32_bf16 v[176:179], v[148:151], v[48:51], v[176:179]\n"
"buffer_load_dwordx4 v138, s[36:39], s58 offen offset:2048 lds\n"
"s_mul_i32 s50, s50, 0x4c957f2d\n"
"ds_read_b128 v[72:75], v187 offset:0\n"
"ds_read_b64_tr_b16 v[208:209], v252 offset:3072\n"
"v_mfma_f32_16x16x32_bf16 v[180:183], v[148:151], v[52:55], v[180:183]\n"
"buffer_load_dwordx4 v139, s[36:39], s58 offen offset:3072 lds\n"
"s_add_u32 s58, s58, 128\n"
"s_cmp_eq_u32 s58, 1152\n"
"s_cselect_b32 s58, 0, s58\n"
"ds_read_b64_tr_b16 v[210:211], v252 offset:3584\n"
"ds_read_b128 v[76:79], v187 offset:2048\n"
"v_mfma_f32_16x16x32_bf16 v[168:171], v[152:155], v[56:59], v[168:171]\n"
"s_add_u32 s50, s50, 0xf767814f\n"
"s_addc_u32 s51, s51, 0x14057b7e\n"
"ds_read_b64_tr_b16 v[212:213], v188 offset:3072\n"
"ds_read_b64_tr_b16 v[214:215], v188 offset:3584\n"
"v_mfma_f32_16x16x32_bf16 v[172:175], v[152:155], v[60:63], v[172:175]\n"
"v_mfma_f32_16x16x32_bf16 v[176:179], v[156:159], v[56:59], v[176:179]\n"
"v_mfma_f32_16x16x32_bf16 v[180:183], v[156:159], v[60:63], v[180:183]\n"
"s_waitcnt lgkmcnt(0)\n"
"s_waitcnt vmcnt(8)\n"
"s_barrier\n"
"ds_read_b128 v[80:83], v184 offset:0\n"
"ds_read_b64_tr_b16 v[144:145], v252 offset:4096\n"
"v_mfma_f32_16x16x32_bf16 v[168:171], v[200:203], v[64:67], v[168:171]\n"
"s_add_u32 m0, s57, 122880\n"
"s_nop 0\n"
"buffer_load_dwordx4 v136, s[36:39], s58 offen lds\n"
"s_xor_b32 s60, s47, 0\n"
"ds_read_b64_tr_b16 v[146:147], v252 offset:4608\n"
"ds_read_b128 v[84:87], v184 offset:2048\n"
"v_mfma_f32_16x16x32_bf16 v[172:175], v[200:203], v[68:71], v[172:175]\n"
"buffer_load_dwordx4 v137, s[36:39], s58 offen offset:1024 lds\n"
"ds_read_b64_tr_b16 v[148:149], v188 offset:4096\n"
"ds_read_b64_tr_b16 v[150:151], v188 offset:4608\n"
"v_mfma_f32_16x16x32_bf16 v[176:179], v[204:207], v[64:67], v[176:179]\n"
"buffer_load_dwordx4 v138, s[36:39], s58 offen offset:2048 lds\n"
"s_lshl_b32 s60, s60, 3\n"
"ds_read_b128 v[88:91], v185 offset:0\n"
"ds_read_b64_tr_b16 v[152:153], v252 offset:5120\n"
"v_mfma_f32_16x16x32_bf16 v[180:183], v[204:207], v[68:71], v[180:183]\n"
"buffer_load_dwordx4 v139, s[36:39], s58 offen offset:3072 lds\n"
"s_add_u32 s58, s58, 128\n"
"s_cmp_eq_u32 s58, 1152\n"
"s_cselect_b32 s58, 0, s58\n"
"ds_read_b64_tr_b16 v[154:155], v252 offset:5632\n"
"ds_read_b128 v[92:95], v185 offset:2048\n"
"v_mfma_f32_16x16x32_bf16 v[168:171], v[208:211], v[72:75], v[168:171]\n"
"ds_read_b64_tr_b16 v[156:157], v188 offset:5120\n"
"ds_read_b64_tr_b16 v[158:159], v188 offset:5632\n"
"v_mfma_f32_16x16x32_bf16 v[172:175], v[208:211], v[76:79], v[172:175]\n"
"s_add_u32 s60, s60, s48\n"
"v_mfma_f32_16x16x32_bf16 v[176:179], v[212:215], v[72:75], v[176:179]\n"
"v_mfma_f32_16x16x32_bf16 v[180:183], v[212:215], v[76:79], v[180:183]\n"
"s_lshl_b32 s60, s60, 2\n"
"s_waitcnt lgkmcnt(0)\n"
"s_waitcnt vmcnt(8)\n"
"s_barrier\n"
"ds_read_b128 v[96:99], v184 offset:24576\n"
"ds_read_b64_tr_b16 v[200:201], v252 offset:24576\n"
"v_mfma_f32_16x16x32_bf16 v[168:171], v[144:147], v[80:83], v[168:171]\n"
"s_mov_b64 s[36:37], s[14:15]\n"
"s_mov_b32 s38, 1152\n"
"s_add_u32 m0, s59, 155648\n"
"s_nop 0\n"
"buffer_load_dwordx4 v240, s[36:39], 0 offen lds\n"
"ds_read_b64_tr_b16 v[202:203], v252 offset:25088\n"
"ds_read_b128 v[100:103], v184 offset:26624\n"
"v_mfma_f32_16x16x32_bf16 v[172:175], v[144:147], v[84:87], v[172:175]\n"
"s_add_u32 s60, s60, s49\n"
"ds_read_b64_tr_b16 v[204:205], v188 offset:24576\n"
"ds_read_b64_tr_b16 v[206:207], v188 offset:25088\n"
"v_mfma_f32_16x16x32_bf16 v[176:179], v[148:151], v[80:83], v[176:179]\n"
"ds_read_b128 v[104:107], v185 offset:24576\n"
"ds_read_b64_tr_b16 v[208:209], v252 offset:25600\n"
"v_mfma_f32_16x16x32_bf16 v[180:183], v[148:151], v[84:87], v[180:183]\n"
"s_lshl_b32 s60, s60, 4\n"
"ds_read_b64_tr_b16 v[210:211], v252 offset:26112\n"
"ds_read_b128 v[108:111], v185 offset:26624\n"
"v_mfma_f32_16x16x32_bf16 v[168:171], v[152:155], v[88:91], v[168:171]\n"
"ds_read_b64_tr_b16 v[212:213], v188 offset:25600\n"
"ds_read_b64_tr_b16 v[214:215], v188 offset:26112\n"
"v_mfma_f32_16x16x32_bf16 v[172:175], v[152:155], v[92:95], v[172:175]\n"
"s_add_u32 s100, s60, 49152\n"
"v_mfma_f32_16x16x32_bf16 v[176:179], v[156:159], v[88:91], v[176:179]\n"
"v_mfma_f32_16x16x32_bf16 v[180:183], v[156:159], v[92:95], v[180:183]\n"
"s_waitcnt lgkmcnt(0)\n"
"s_waitcnt vmcnt(5)\n"
"s_barrier\n"
"ds_read_b128 v[112:115], v186 offset:16384\n"
"ds_read_b64_tr_b16 v[144:145], v252 offset:26624\n"
"v_mfma_f32_16x16x32_bf16 v[168:171], v[200:203], v[96:99], v[168:171]\n"
"s_mul_i32 s60, s47, 9\n"
"s_add_u32 s60, s60, s48\n"
"s_lshl_b32 s60, s60, 16\n"
"s_lshl_b32 s61, s49, 11\n"
"s_add_u32 s60, s60, s61\n"
"s_add_u32 s62, s60, 16842752\n"
"v_mov_b32 v240, -1\n"
"v_mov_b32 v241, -1\n"
"v_mov_b32 v242, -1\n"
"v_mov_b32 v243, -1\n"
"s_add_u32 s61, s48, 1\n"
"s_and_b32 s61, s61, 7\n"
"s_lshl_b32 s61, s61, 13\n"
"s_add_u32 s60, s62, s61\n"
"buffer_store_dwordx4 v[240:243], v255, s[28:31], s60 offen sc1\n"
"ds_read_b64_tr_b16 v[146:147], v252 offset:27136\n"
"ds_read_b128 v[116:119], v186 offset:18432\n"
"v_mfma_f32_16x16x32_bf16 v[172:175], v[200:203], v[100:103], v[172:175]\n"
"buffer_store_dwordx4 v[240:243], v255, s[28:31], s60 offen offset:1024 sc1\n"
"ds_read_b64_tr_b16 v[148:149], v188 offset:26624\n"
"ds_read_b64_tr_b16 v[150:151], v188 offset:27136\n"
"v_mfma_f32_16x16x32_bf16 v[176:179], v[204:207], v[96:99], v[176:179]\n"
"ds_read_b128 v[120:123], v187 offset:16384\n"
"ds_read_b64_tr_b16 v[152:153], v252 offset:27648\n"
"v_mfma_f32_16x16x32_bf16 v[180:183], v[204:207], v[100:103], v[180:183]\n"
"ds_read_b64_tr_b16 v[154:155], v252 offset:28160\n"
"ds_read_b128 v[124:127], v187 offset:18432\n"
"v_mfma_f32_16x16x32_bf16 v[168:171], v[208:211], v[104:107], v[168:171]\n"
"ds_read_b64_tr_b16 v[156:157], v188 offset:27648\n"
"ds_read_b64_tr_b16 v[158:159], v188 offset:28160\n"
"v_mfma_f32_16x16x32_bf16 v[172:175], v[208:211], v[108:111], v[172:175]\n"
"v_mfma_f32_16x16x32_bf16 v[176:179], v[212:215], v[104:107], v[176:179]\n"
"v_mfma_f32_16x16x32_bf16 v[180:183], v[212:215], v[108:111], v[180:183]\n"
"s_waitcnt lgkmcnt(0)\n"
"s_waitcnt vmcnt(3)\n"
"s_barrier\n"
"ds_read_b128 v[128:131], v186 offset:0\n"
"ds_read_b64_tr_b16 v[200:201], v252 offset:28672\n"
"v_mfma_f32_16x16x32_bf16 v[168:171], v[144:147], v[112:115], v[168:171]\n"
"s_cmp_eq_u32 s49, 0\n"
"s_mov_b32 s60, 18432\n"
"s_cselect_b32 s60, s60, 0x40000000\n"
"v_mov_b32 v240, 0x3f803f80\n"
"v_mov_b32 v241, 0x3f803f80\n"
"v_mov_b32 v242, 0x3f803f80\n"
"v_mov_b32 v243, 0x3f803f80\n"
"ds_read_b64_tr_b16 v[202:203], v252 offset:29184\n"
"ds_read_b128 v[132:135], v186 offset:2048\n"
"v_mfma_f32_16x16x32_bf16 v[172:175], v[144:147], v[116:119], v[172:175]\n"
"buffer_store_dwordx4 v[240:243], v255, s[20:23], s60 offen sc1\n"
"ds_read_b64_tr_b16 v[204:205], v188 offset:28672\n"
"ds_read_b64_tr_b16 v[206:207], v188 offset:29184\n"
"v_mfma_f32_16x16x32_bf16 v[176:179], v[148:151], v[112:115], v[176:179]\n"
"ds_read_b128 v[136:139], v187 offset:0\n"
"ds_read_b64_tr_b16 v[208:209], v252 offset:29696\n"
"v_mfma_f32_16x16x32_bf16 v[180:183], v[148:151], v[116:119], v[180:183]\n"
"ds_read_b64_tr_b16 v[210:211], v252 offset:30208\n"
"ds_read_b128 v[140:143], v187 offset:2048\n"
"v_mfma_f32_16x16x32_bf16 v[168:171], v[152:155], v[120:123], v[168:171]\n"
"ds_read_b64_tr_b16 v[212:213], v188 offset:29696\n"
"ds_read_b64_tr_b16 v[214:215], v188 offset:30208\n"
"v_mfma_f32_16x16x32_bf16 v[172:175], v[152:155], v[124:127], v[172:175]\n"
"v_mfma_f32_16x16x32_bf16 v[176:179], v[156:159], v[120:123], v[176:179]\n"
"v_mfma_f32_16x16x32_bf16 v[180:183], v[156:159], v[124:127], v[180:183]\n"
"v_xor_b32 v252, s35, v252\n"
"v_add_u32 v253, 49152, v252\n"
"v_xor_b32 v254, 32, v253\n"
"s_mov_b32 s42, 73728\n"
"s_mov_b32 s43, 24576\n"
"s_mov_b32 s44, 49152\n"
"s_waitcnt lgkmcnt(0)\n"
"s_waitcnt vmcnt(3)\n"
"s_barrier\n"
"ds_read_b64_tr_b16 v[144:145], v253 offset:0\n"
"ds_read_b64_tr_b16 v[146:147], v253 offset:512\n"
"v_mfma_f32_16x16x32_bf16 v[168:171], v[200:203], v[128:131], v[168:171]\n"
"s_lshl2_add_u32 s60, s47, s49\n"
"s_sub_u32 s61, s60, 108\n"
"s_cselect_b32 s60, s60, s61\n"
"s_cselect_b64 s[36:37], s[12:13], s[8:9]\n"
"s_cselect_b32 s38, 8, 1\n"
"ds_read_b64_tr_b16 v[148:149], v254 offset:0\n"
"ds_read_b64_tr_b16 v[150:151], v254 offset:512\n"
"v_mfma_f32_16x16x32_bf16 v[172:175], v[200:203], v[132:135], v[172:175]\n"
"s_mul_i32 s38, s38, 82944\n"
"s_mul_i32 s61, s38, s48\n"
"s_add_u32 s36, s36, s61\n"
"s_addc_u32 s37, s37, 0\n"
"ds_read_b64_tr_b16 v[152:153], v253 offset:1024\n"
"ds_read_b64_tr_b16 v[154:155], v253 offset:1536\n"
"v_mfma_f32_16x16x32_bf16 v[176:179], v[204:207], v[128:131], v[176:179]\n"
"s_mul_i32 s60, s60, 6144\n"
"v_lshl_add_u32 v251, v255, 3, s60\n"
"ds_read_b64_tr_b16 v[156:157], v254 offset:1024\n"
"ds_read_b64_tr_b16 v[158:159], v254 offset:1536\n"
"v_mfma_f32_16x16x32_bf16 v[180:183], v[204:207], v[132:135], v[180:183]\n"
"s_movk_i32 s58, 2048\n"
"v_cmp_gt_u32 vcc, 256, v255\n"
"v_bfrev_b32 v240, 2\n"
"ds_read_b64_tr_b16 v[160:161], v253 offset:2048\n"
"ds_read_b64_tr_b16 v[162:163], v253 offset:2560\n"
"v_mfma_f32_16x16x32_bf16 v[184:187], v[208:211], v[136:139], v[168:171]\n"
"v_cndmask_b32 v251, v240, v251, vcc\n"
"ds_read_b64_tr_b16 v[164:165], v254 offset:2048\n"
"ds_read_b64_tr_b16 v[166:167], v254 offset:2560\n"
"v_mfma_f32_16x16x32_bf16 v[188:191], v[208:211], v[140:143], v[172:175]\n"
"v_mfma_f32_16x16x32_bf16 v[192:195], v[212:215], v[136:139], v[176:179]\n"
"v_mfma_f32_16x16x32_bf16 v[196:199], v[212:215], v[140:143], v[180:183]\n"
"s_mov_b32 s45, 2\n"
"s_cmp_ge_u32 s48, 3\n"
"s_cbranch_scc1 LHBH_%=\n"
"LQH_%=:\n"
"s_waitcnt lgkmcnt(8)\n"
"v_mfma_f32_16x16x32_bf16 v[168:171], v[144:147], v[0:3], 0\n"
"v_mfma_f32_16x16x32_bf16 v[172:175], v[144:147], v[4:7], 0\n"
"ds_read_b64_tr_b16 v[144:145], v253 offset:3072\n"
"v_mfma_f32_16x16x32_bf16 v[176:179], v[148:151], v[0:3], 0\n"
"ds_read_b64_tr_b16 v[146:147], v253 offset:3584\n"
"v_mfma_f32_16x16x32_bf16 v[180:183], v[148:151], v[4:7], 0\n"
"ds_read_b64_tr_b16 v[148:149], v254 offset:3072\n"
"s_waitcnt lgkmcnt(7)\n"
"v_mfma_f32_16x16x32_bf16 v[168:171], v[152:155], v[8:11], v[168:171]\n"
"ds_read_b64_tr_b16 v[150:151], v254 offset:3584\n"
"v_mfma_f32_16x16x32_bf16 v[172:175], v[152:155], v[12:15], v[172:175]\n"
"ds_read_b64_tr_b16 v[152:153], v253 offset:4096\n"
"v_mfma_f32_16x16x32_bf16 v[176:179], v[156:159], v[8:11], v[176:179]\n"
"ds_read_b64_tr_b16 v[154:155], v253 offset:4608\n"
"v_mfma_f32_16x16x32_bf16 v[180:183], v[156:159], v[12:15], v[180:183]\n"
"ds_read_b64_tr_b16 v[156:157], v254 offset:4096\n"
"v_mul_f32 v184, s54, v184\n"
"v_mul_f32 v185, s54, v185\n"
"v_mul_f32 v186, s54, v186\n"
"v_mul_f32 v187, s54, v187\n"
"s_waitcnt lgkmcnt(7)\n"
"v_mfma_f32_16x16x32_bf16 v[168:171], v[160:163], v[16:19], v[168:171]\n"
"ds_read_b64_tr_b16 v[158:159], v254 offset:4608\n"
"v_mfma_f32_16x16x32_bf16 v[172:175], v[160:163], v[20:23], v[172:175]\n"
"ds_read_b64_tr_b16 v[160:161], v253 offset:5120\n"
"v_mfma_f32_16x16x32_bf16 v[176:179], v[164:167], v[16:19], v[176:179]\n"
"ds_read_b64_tr_b16 v[162:163], v253 offset:5632\n"
"v_mfma_f32_16x16x32_bf16 v[180:183], v[164:167], v[20:23], v[180:183]\n"
"ds_read_b64_tr_b16 v[164:165], v254 offset:5120\n"
"ds_read_b64_tr_b16 v[166:167], v254 offset:5632\n"
"v_add_u32 v253, s42, v252\n"
"v_xor_b32 v254, 32, v253\n"
"v_mul_f32 v188, s54, v188\n"
"v_mul_f32 v189, s54, v189\n"
"v_mul_f32 v190, s54, v190\n"
"v_mul_f32 v191, s54, v191\n"
"s_waitcnt lgkmcnt(8)\n"
"v_mfma_f32_16x16x32_bf16 v[168:171], v[144:147], v[24:27], v[168:171]\n"
"v_mfma_f32_16x16x32_bf16 v[172:175], v[144:147], v[28:31], v[172:175]\n"
"s_barrier\n"
"ds_read_b64_tr_b16 v[144:145], v253 offset:0\n"
"v_mfma_f32_16x16x32_bf16 v[176:179], v[148:151], v[24:27], v[176:179]\n"
"ds_read_b64_tr_b16 v[146:147], v253 offset:512\n"
"v_mfma_f32_16x16x32_bf16 v[180:183], v[148:151], v[28:31], v[180:183]\n"
"ds_read_b64_tr_b16 v[148:149], v254 offset:0\n"
"v_cvt_pk_bf16_f32 v208, v184, v185\n"
"v_cvt_pk_bf16_f32 v209, v186, v187\n"
"v_cvt_pk_bf16_f32 v210, v188, v189\n"
"v_cvt_pk_bf16_f32 v211, v190, v191\n"
"s_waitcnt lgkmcnt(7)\n"
"v_mfma_f32_16x16x32_bf16 v[168:171], v[152:155], v[32:35], v[168:171]\n"
"ds_read_b64_tr_b16 v[150:151], v254 offset:512\n"
"s_add_u32 s43, s42, 24576\n"
"v_mfma_f32_16x16x32_bf16 v[172:175], v[152:155], v[36:39], v[172:175]\n"
"ds_read_b64_tr_b16 v[152:153], v253 offset:1024\n"
"s_cmp_eq_u32 s43, 122880\n"
"v_mfma_f32_16x16x32_bf16 v[176:179], v[156:159], v[32:35], v[176:179]\n"
"ds_read_b64_tr_b16 v[154:155], v253 offset:1536\n"
"s_cselect_b32 s43, 0, s43\n"
"v_mfma_f32_16x16x32_bf16 v[180:183], v[156:159], v[36:39], v[180:183]\n"
"ds_read_b64_tr_b16 v[156:157], v254 offset:1024\n"
"v_permlane16_swap_b32 v208, v210\n"
"v_permlane16_swap_b32 v209, v211\n"
"buffer_store_dwordx4 v[208:211], v245, s[28:31], s52 offen sc1\n"
"s_waitcnt lgkmcnt(7)\n"
"v_mfma_f32_16x16x32_bf16 v[168:171], v[160:163], v[40:43], v[168:171]\n"
"ds_read_b64_tr_b16 v[158:159], v254 offset:1536\n"
"v_mfma_f32_16x16x32_bf16 v[172:175], v[160:163], v[44:47], v[172:175]\n"
"ds_read_b64_tr_b16 v[160:161], v253 offset:2048\n"
"v_mfma_f32_16x16x32_bf16 v[176:179], v[164:167], v[40:43], v[176:179]\n"
"ds_read_b64_tr_b16 v[162:163], v253 offset:2560\n"
"v_mfma_f32_16x16x32_bf16 v[180:183], v[164:167], v[44:47], v[180:183]\n"
"ds_read_b64_tr_b16 v[164:165], v254 offset:2048\n"
"ds_read_b64_tr_b16 v[166:167], v254 offset:2560\n"
"v_mul_f32 v192, s54, v192\n"
"v_mul_f32 v193, s54, v193\n"
"v_mul_f32 v194, s54, v194\n"
"v_mul_f32 v195, s54, v195\n"
"s_waitcnt lgkmcnt(8)\n"
"v_mfma_f32_16x16x32_bf16 v[168:171], v[144:147], v[48:51], v[168:171]\n"
"v_mfma_f32_16x16x32_bf16 v[172:175], v[144:147], v[52:55], v[172:175]\n"
"ds_read_b64_tr_b16 v[144:145], v253 offset:3072\n"
"v_mfma_f32_16x16x32_bf16 v[176:179], v[148:151], v[48:51], v[176:179]\n"
"ds_read_b64_tr_b16 v[146:147], v253 offset:3584\n"
"v_mfma_f32_16x16x32_bf16 v[180:183], v[148:151], v[52:55], v[180:183]\n"
"ds_read_b64_tr_b16 v[148:149], v254 offset:3072\n"
"v_mul_f32 v196, s54, v196\n"
"v_mul_f32 v197, s54, v197\n"
"v_mul_f32 v198, s54, v198\n"
"v_mul_f32 v199, s54, v199\n"
"s_waitcnt lgkmcnt(7)\n"
"v_mfma_f32_16x16x32_bf16 v[168:171], v[152:155], v[56:59], v[168:171]\n"
"ds_read_b64_tr_b16 v[150:151], v254 offset:3584\n"
"v_mfma_f32_16x16x32_bf16 v[172:175], v[152:155], v[60:63], v[172:175]\n"
"ds_read_b64_tr_b16 v[152:153], v253 offset:4096\n"
"v_mfma_f32_16x16x32_bf16 v[176:179], v[156:159], v[56:59], v[176:179]\n"
"ds_read_b64_tr_b16 v[154:155], v253 offset:4608\n"
"v_mfma_f32_16x16x32_bf16 v[180:183], v[156:159], v[60:63], v[180:183]\n"
"ds_read_b64_tr_b16 v[156:157], v254 offset:4096\n"
"v_cvt_pk_bf16_f32 v212, v192, v193\n"
"v_cvt_pk_bf16_f32 v213, v194, v195\n"
"v_cvt_pk_bf16_f32 v214, v196, v197\n"
"v_cvt_pk_bf16_f32 v215, v198, v199\n"
"s_waitcnt lgkmcnt(7)\n"
"v_mfma_f32_16x16x32_bf16 v[168:171], v[160:163], v[64:67], v[168:171]\n"
"ds_read_b64_tr_b16 v[158:159], v254 offset:4608\n"
"v_mfma_f32_16x16x32_bf16 v[172:175], v[160:163], v[68:71], v[172:175]\n"
"ds_read_b64_tr_b16 v[160:161], v253 offset:5120\n"
"v_mfma_f32_16x16x32_bf16 v[176:179], v[164:167], v[64:67], v[176:179]\n"
"ds_read_b64_tr_b16 v[162:163], v253 offset:5632\n"
"v_mfma_f32_16x16x32_bf16 v[180:183], v[164:167], v[68:71], v[180:183]\n"
"ds_read_b64_tr_b16 v[164:165], v254 offset:5120\n"
"ds_read_b64_tr_b16 v[166:167], v254 offset:5632\n"
"v_add_u32 v253, s43, v252\n"
"v_xor_b32 v254, 32, v253\n"
"v_permlane16_swap_b32 v212, v214\n"
"v_permlane16_swap_b32 v213, v215\n"
"buffer_store_dwordx4 v[212:215], v246, s[28:31], s52 offen sc1\n"
"s_waitcnt lgkmcnt(8)\n"
"v_mfma_f32_16x16x32_bf16 v[168:171], v[144:147], v[72:75], v[168:171]\n"
"v_mfma_f32_16x16x32_bf16 v[172:175], v[144:147], v[76:79], v[172:175]\n"
"s_barrier\n"
"ds_read_b64_tr_b16 v[144:145], v253 offset:0\n"
"s_cmp_eq_u32 s45, 1\n"
"v_mfma_f32_16x16x32_bf16 v[176:179], v[148:151], v[72:75], v[176:179]\n"
"ds_read_b64_tr_b16 v[146:147], v253 offset:512\n"
"s_cselect_b32 s60, 0, 0x40000000\n"
"v_mfma_f32_16x16x32_bf16 v[180:183], v[148:151], v[76:79], v[180:183]\n"
"ds_read_b64_tr_b16 v[148:149], v254 offset:0\n"
"v_mov_b32 v245, v247\n"
"v_mov_b32 v246, v248\n"
"s_mov_b32 s52, s53\n"
"s_mov_b32 s54, s55\n"
"s_waitcnt lgkmcnt(7)\n"
"v_mfma_f32_16x16x32_bf16 v[168:171], v[152:155], v[80:83], v[168:171]\n"
"ds_read_b64_tr_b16 v[150:151], v254 offset:512\n"
"s_add_u32 s44, s43, 24576\n"
"v_mfma_f32_16x16x32_bf16 v[172:175], v[152:155], v[84:87], v[172:175]\n"
"ds_read_b64_tr_b16 v[152:153], v253 offset:1024\n"
"s_cmp_eq_u32 s44, 122880\n"
"v_mfma_f32_16x16x32_bf16 v[176:179], v[156:159], v[80:83], v[176:179]\n"
"ds_read_b64_tr_b16 v[154:155], v253 offset:1536\n"
"s_cselect_b32 s44, 0, s44\n"
"v_mfma_f32_16x16x32_bf16 v[180:183], v[156:159], v[84:87], v[180:183]\n"
"ds_read_b64_tr_b16 v[156:157], v254 offset:1024\n"
"buffer_load_dword v239, v251, s[36:39], s60 offen\n"
"s_waitcnt lgkmcnt(7)\n"
"v_mfma_f32_16x16x32_bf16 v[168:171], v[160:163], v[88:91], v[168:171]\n"
"ds_read_b64_tr_b16 v[158:159], v254 offset:1536\n"
"v_mfma_f32_16x16x32_bf16 v[172:175], v[160:163], v[92:95], v[172:175]\n"
"ds_read_b64_tr_b16 v[160:161], v253 offset:2048\n"
"v_mfma_f32_16x16x32_bf16 v[176:179], v[164:167], v[88:91], v[176:179]\n"
"ds_read_b64_tr_b16 v[162:163], v253 offset:2560\n"
"v_mfma_f32_16x16x32_bf16 v[180:183], v[164:167], v[92:95], v[180:183]\n"
"ds_read_b64_tr_b16 v[164:165], v254 offset:2048\n"
"ds_read_b64_tr_b16 v[166:167], v254 offset:2560\n"
"s_waitcnt lgkmcnt(8)\n"
"v_mfma_f32_16x16x32_bf16 v[168:171], v[144:147], v[96:99], v[168:171]\n"
"v_mfma_f32_16x16x32_bf16 v[172:175], v[144:147], v[100:103], v[172:175]\n"
"ds_read_b64_tr_b16 v[144:145], v253 offset:3072\n"
"v_mfma_f32_16x16x32_bf16 v[176:179], v[148:151], v[96:99], v[176:179]\n"
"ds_read_b64_tr_b16 v[146:147], v253 offset:3584\n"
"v_mfma_f32_16x16x32_bf16 v[180:183], v[148:151], v[100:103], v[180:183]\n"
"ds_read_b64_tr_b16 v[148:149], v254 offset:3072\n"
"s_waitcnt lgkmcnt(7)\n"
"v_mfma_f32_16x16x32_bf16 v[168:171], v[152:155], v[104:107], v[168:171]\n"
"ds_read_b64_tr_b16 v[150:151], v254 offset:3584\n"
"v_mfma_f32_16x16x32_bf16 v[172:175], v[152:155], v[108:111], v[172:175]\n"
"ds_read_b64_tr_b16 v[152:153], v253 offset:4096\n"
"v_mfma_f32_16x16x32_bf16 v[176:179], v[156:159], v[104:107], v[176:179]\n"
"ds_read_b64_tr_b16 v[154:155], v253 offset:4608\n"
"v_mfma_f32_16x16x32_bf16 v[180:183], v[156:159], v[108:111], v[180:183]\n"
"ds_read_b64_tr_b16 v[156:157], v254 offset:4096\n"
"s_waitcnt lgkmcnt(7)\n"
"v_mfma_f32_16x16x32_bf16 v[168:171], v[160:163], v[112:115], v[168:171]\n"
"ds_read_b64_tr_b16 v[158:159], v254 offset:4608\n"
"v_mfma_f32_16x16x32_bf16 v[172:175], v[160:163], v[116:119], v[172:175]\n"
"ds_read_b64_tr_b16 v[160:161], v253 offset:5120\n"
"v_mfma_f32_16x16x32_bf16 v[176:179], v[164:167], v[112:115], v[176:179]\n"
"ds_read_b64_tr_b16 v[162:163], v253 offset:5632\n"
"v_mfma_f32_16x16x32_bf16 v[180:183], v[164:167], v[116:119], v[180:183]\n"
"ds_read_b64_tr_b16 v[164:165], v254 offset:5120\n"
"ds_read_b64_tr_b16 v[166:167], v254 offset:5632\n"
"v_add_u32 v253, s44, v252\n"
"v_xor_b32 v254, 32, v253\n"
"s_waitcnt lgkmcnt(8)\n"
"v_mfma_f32_16x16x32_bf16 v[168:171], v[144:147], v[120:123], v[168:171]\n"
"v_mfma_f32_16x16x32_bf16 v[172:175], v[144:147], v[124:127], v[172:175]\n"
"s_barrier\n"
"ds_read_b64_tr_b16 v[144:145], v253 offset:0\n"
"v_mfma_f32_16x16x32_bf16 v[176:179], v[148:151], v[120:123], v[176:179]\n"
"ds_read_b64_tr_b16 v[146:147], v253 offset:512\n"
"v_mfma_f32_16x16x32_bf16 v[180:183], v[148:151], v[124:127], v[180:183]\n"
"ds_read_b64_tr_b16 v[148:149], v254 offset:0\n"
"s_waitcnt lgkmcnt(7)\n"
"v_mfma_f32_16x16x32_bf16 v[168:171], v[152:155], v[128:131], v[168:171]\n"
"ds_read_b64_tr_b16 v[150:151], v254 offset:512\n"
"s_add_u32 s42, s44, 24576\n"
"v_mfma_f32_16x16x32_bf16 v[172:175], v[152:155], v[132:135], v[172:175]\n"
"ds_read_b64_tr_b16 v[152:153], v253 offset:1024\n"
"s_cmp_eq_u32 s42, 122880\n"
"v_mfma_f32_16x16x32_bf16 v[176:179], v[156:159], v[128:131], v[176:179]\n"
"ds_read_b64_tr_b16 v[154:155], v253 offset:1536\n"
"s_cselect_b32 s42, 0, s42\n"
"v_mfma_f32_16x16x32_bf16 v[180:183], v[156:159], v[132:135], v[180:183]\n"
"ds_read_b64_tr_b16 v[156:157], v254 offset:1024\n"
"s_waitcnt lgkmcnt(7)\n"
"v_mfma_f32_16x16x32_bf16 v[184:187], v[160:163], v[136:139], v[168:171]\n"
"ds_read_b64_tr_b16 v[158:159], v254 offset:1536\n"
"s_sub_u32 s45, s45, 1\n"
"v_mfma_f32_16x16x32_bf16 v[188:191], v[160:163], v[140:143], v[172:175]\n"
"ds_read_b64_tr_b16 v[160:161], v253 offset:2048\n"
"s_cmp_lg_u32 s45, 0\n"
"v_mfma_f32_16x16x32_bf16 v[192:195], v[164:167], v[136:139], v[176:179]\n"
"ds_read_b64_tr_b16 v[162:163], v253 offset:2560\n"
"v_mfma_f32_16x16x32_bf16 v[196:199], v[164:167], v[140:143], v[180:183]\n"
"ds_read_b64_tr_b16 v[164:165], v254 offset:2048\n"
"ds_read_b64_tr_b16 v[166:167], v254 offset:2560\n"
"s_cbranch_scc1 LQH_%=\n"
"s_mov_b32 s45, 3\n"
"s_lshl_b32 s35, s34, 5\n"
"s_cmp_eq_u32 s46, 4\n"
"s_cselect_b32 s68, 3, 0\n"
"s_cmp_eq_u32 s46, 4\n"
"s_cselect_b32 s69, 2, 0\n"
"LSH_%=:\n"
"s_waitcnt lgkmcnt(8)\n"
"v_mfma_f32_16x16x32_bf16 v[168:171], v[144:147], v[0:3], 0\n"
"v_mfma_f32_16x16x32_bf16 v[172:175], v[144:147], v[4:7], 0\n"
"ds_read_b64_tr_b16 v[144:145], v253 offset:3072\n"
"v_mfma_f32_16x16x32_bf16 v[176:179], v[148:151], v[0:3], 0\n"
"ds_read_b64_tr_b16 v[146:147], v253 offset:3584\n"
"v_mfma_f32_16x16x32_bf16 v[180:183], v[148:151], v[4:7], 0\n"
"ds_read_b64_tr_b16 v[148:149], v254 offset:3072\n"
"v_mul_f32 v200, s56, v184\n"
"v_exp_f32 v200, v200\n"
"v_mul_f32 v201, s56, v185\n"
"v_add_f32 v200, 1.0, v200\n"
"v_exp_f32 v201, v201\n"
"v_mul_f32 v202, s56, v186\n"
"s_waitcnt lgkmcnt(7)\n"
"v_mfma_f32_16x16x32_bf16 v[168:171], v[152:155], v[8:11], v[168:171]\n"
"ds_read_b64_tr_b16 v[150:151], v254 offset:3584\n"
"v_mfma_f32_16x16x32_bf16 v[172:175], v[152:155], v[12:15], v[172:175]\n"
"ds_read_b64_tr_b16 v[152:153], v253 offset:4096\n"
"v_mfma_f32_16x16x32_bf16 v[176:179], v[156:159], v[8:11], v[176:179]\n"
"ds_read_b64_tr_b16 v[154:155], v253 offset:4608\n"
"v_mfma_f32_16x16x32_bf16 v[180:183], v[156:159], v[12:15], v[180:183]\n"
"ds_read_b64_tr_b16 v[156:157], v254 offset:4096\n"
"v_rcp_f32 v200, v200\n"
"v_add_f32 v201, 1.0, v201\n"
"v_mul_f32 v184, v184, v200\n"
"v_exp_f32 v202, v202\n"
"v_mul_f32 v200, s56, v187\n"
"v_rcp_f32 v201, v201\n"
"s_waitcnt lgkmcnt(7)\n"
"v_mfma_f32_16x16x32_bf16 v[168:171], v[160:163], v[16:19], v[168:171]\n"
"ds_read_b64_tr_b16 v[158:159], v254 offset:4608\n"
"v_mfma_f32_16x16x32_bf16 v[172:175], v[160:163], v[20:23], v[172:175]\n"
"ds_read_b64_tr_b16 v[160:161], v253 offset:5120\n"
"v_mfma_f32_16x16x32_bf16 v[176:179], v[164:167], v[16:19], v[176:179]\n"
"ds_read_b64_tr_b16 v[162:163], v253 offset:5632\n"
"v_mfma_f32_16x16x32_bf16 v[180:183], v[164:167], v[20:23], v[180:183]\n"
"ds_read_b64_tr_b16 v[164:165], v254 offset:5120\n"
"ds_read_b64_tr_b16 v[166:167], v254 offset:5632\n"
"v_add_u32 v253, s42, v252\n"
"v_xor_b32 v254, 32, v253\n"
"v_add_f32 v202, 1.0, v202\n"
"v_mul_f32 v185, v185, v201\n"
"v_cvt_pk_bf16_f32 v208, v184, v185\n"
"v_exp_f32 v200, v200\n"
"s_waitcnt lgkmcnt(8)\n"
"v_mfma_f32_16x16x32_bf16 v[168:171], v[144:147], v[24:27], v[168:171]\n"
"v_mfma_f32_16x16x32_bf16 v[172:175], v[144:147], v[28:31], v[172:175]\n"
"s_barrier\n"
"ds_read_b64_tr_b16 v[144:145], v253 offset:0\n"
"v_mfma_f32_16x16x32_bf16 v[176:179], v[148:151], v[24:27], v[176:179]\n"
"ds_read_b64_tr_b16 v[146:147], v253 offset:512\n"
"v_mfma_f32_16x16x32_bf16 v[180:183], v[148:151], v[28:31], v[180:183]\n"
"ds_read_b64_tr_b16 v[148:149], v254 offset:0\n"
"v_mul_f32 v201, s56, v192\n"
"v_rcp_f32 v202, v202\n"
"v_add_f32 v200, 1.0, v200\n"
"v_mul_f32 v186, v186, v202\n"
"v_exp_f32 v201, v201\n"
"v_mul_f32 v202, s56, v193\n"
"s_waitcnt lgkmcnt(7)\n"
"v_mfma_f32_16x16x32_bf16 v[168:171], v[152:155], v[32:35], v[168:171]\n"
"ds_read_b64_tr_b16 v[150:151], v254 offset:512\n"
"s_add_u32 s43, s42, 24576\n"
"v_mfma_f32_16x16x32_bf16 v[172:175], v[152:155], v[36:39], v[172:175]\n"
"ds_read_b64_tr_b16 v[152:153], v253 offset:1024\n"
"s_cmp_eq_u32 s43, 122880\n"
"v_mfma_f32_16x16x32_bf16 v[176:179], v[156:159], v[32:35], v[176:179]\n"
"ds_read_b64_tr_b16 v[154:155], v253 offset:1536\n"
"s_cselect_b32 s43, 0, s43\n"
"v_mfma_f32_16x16x32_bf16 v[180:183], v[156:159], v[36:39], v[180:183]\n"
"ds_read_b64_tr_b16 v[156:157], v254 offset:1024\n"
"v_rcp_f32 v200, v200\n"
"v_add_f32 v201, 1.0, v201\n"
"v_mul_f32 v187, v187, v200\n"
"v_cvt_pk_bf16_f32 v209, v186, v187\n"
"v_exp_f32 v202, v202\n"
"s_waitcnt lgkmcnt(7)\n"
"v_mfma_f32_16x16x32_bf16 v[168:171], v[160:163], v[40:43], v[168:171]\n"
"ds_read_b64_tr_b16 v[158:159], v254 offset:1536\n"
"v_mfma_f32_16x16x32_bf16 v[172:175], v[160:163], v[44:47], v[172:175]\n"
"ds_read_b64_tr_b16 v[160:161], v253 offset:2048\n"
"v_mfma_f32_16x16x32_bf16 v[176:179], v[164:167], v[40:43], v[176:179]\n"
"ds_read_b64_tr_b16 v[162:163], v253 offset:2560\n"
"v_mfma_f32_16x16x32_bf16 v[180:183], v[164:167], v[44:47], v[180:183]\n"
"ds_read_b64_tr_b16 v[164:165], v254 offset:2048\n"
"ds_read_b64_tr_b16 v[166:167], v254 offset:2560\n"
"v_mul_f32 v200, s56, v194\n"
"v_rcp_f32 v201, v201\n"
"v_add_f32 v202, 1.0, v202\n"
"v_mul_f32 v192, v192, v201\n"
"v_exp_f32 v200, v200\n"
"v_mul_f32 v201, s56, v195\n"
"s_waitcnt lgkmcnt(8)\n"
"v_mfma_f32_16x16x32_bf16 v[168:171], v[144:147], v[48:51], v[168:171]\n"
"v_mfma_f32_16x16x32_bf16 v[172:175], v[144:147], v[52:55], v[172:175]\n"
"ds_read_b64_tr_b16 v[144:145], v253 offset:3072\n"
"v_mfma_f32_16x16x32_bf16 v[176:179], v[148:151], v[48:51], v[176:179]\n"
"ds_read_b64_tr_b16 v[146:147], v253 offset:3584\n"
"v_mfma_f32_16x16x32_bf16 v[180:183], v[148:151], v[52:55], v[180:183]\n"
"ds_read_b64_tr_b16 v[148:149], v254 offset:3072\n"
"v_rcp_f32 v202, v202\n"
"v_add_f32 v200, 1.0, v200\n"
"v_mul_f32 v193, v193, v202\n"
"v_cvt_pk_bf16_f32 v210, v192, v193\n"
"v_exp_f32 v201, v201\n"
"v_mul_f32 v202, s56, v188\n"
"s_waitcnt lgkmcnt(7)\n"
"v_mfma_f32_16x16x32_bf16 v[168:171], v[152:155], v[56:59], v[168:171]\n"
"ds_read_b64_tr_b16 v[150:151], v254 offset:3584\n"
"v_mfma_f32_16x16x32_bf16 v[172:175], v[152:155], v[60:63], v[172:175]\n"
"ds_read_b64_tr_b16 v[152:153], v253 offset:4096\n"
"v_mfma_f32_16x16x32_bf16 v[176:179], v[156:159], v[56:59], v[176:179]\n"
"ds_read_b64_tr_b16 v[154:155], v253 offset:4608\n"
"v_mfma_f32_16x16x32_bf16 v[180:183], v[156:159], v[60:63], v[180:183]\n"
"ds_read_b64_tr_b16 v[156:157], v254 offset:4096\n"
"v_rcp_f32 v200, v200\n"
"v_add_f32 v201, 1.0, v201\n"
"v_mul_f32 v194, v194, v200\n"
"v_exp_f32 v202, v202\n"
"v_mul_f32 v200, s56, v189\n"
"v_rcp_f32 v201, v201\n"
"s_waitcnt lgkmcnt(7)\n"
"v_mfma_f32_16x16x32_bf16 v[168:171], v[160:163], v[64:67], v[168:171]\n"
"ds_read_b64_tr_b16 v[158:159], v254 offset:4608\n"
"v_mfma_f32_16x16x32_bf16 v[172:175], v[160:163], v[68:71], v[172:175]\n"
"ds_read_b64_tr_b16 v[160:161], v253 offset:5120\n"
"v_mfma_f32_16x16x32_bf16 v[176:179], v[164:167], v[64:67], v[176:179]\n"
"ds_read_b64_tr_b16 v[162:163], v253 offset:5632\n"
"v_mfma_f32_16x16x32_bf16 v[180:183], v[164:167], v[68:71], v[180:183]\n"
"ds_read_b64_tr_b16 v[164:165], v254 offset:5120\n"
"ds_read_b64_tr_b16 v[166:167], v254 offset:5632\n"
"v_add_u32 v253, s43, v252\n"
"v_xor_b32 v254, 32, v253\n"
"v_add_f32 v202, 1.0, v202\n"
"v_mul_f32 v195, v195, v201\n"
"v_cvt_pk_bf16_f32 v211, v194, v195\n"
"buffer_store_dwordx4 v[208:211], v249, s[28:31], s41 offen sc1\n"
"s_waitcnt lgkmcnt(8)\n"
"v_mfma_f32_16x16x32_bf16 v[168:171], v[144:147], v[72:75], v[168:171]\n"
"v_mfma_f32_16x16x32_bf16 v[172:175], v[144:147], v[76:79], v[172:175]\n"
"s_waitcnt vmcnt(1)\n"
"s_cmp_eq_u32 s45, 1\n"
"s_cbranch_scc1 O22_%=\n"
"E21_%=:\n"
"s_barrier\n"
"ds_read_b64_tr_b16 v[144:145], v253 offset:0\n"
"s_cmp_eq_u32 s45, s68\n"
"v_mfma_f32_16x16x32_bf16 v[176:179], v[148:151], v[72:75], v[176:179]\n"
"ds_read_b64_tr_b16 v[146:147], v253 offset:512\n"
"s_cbranch_scc1 O24_%=\n"
"E23_%=:\n"
"v_mfma_f32_16x16x32_bf16 v[180:183], v[148:151], v[76:79], v[180:183]\n"
"ds_read_b64_tr_b16 v[148:149], v254 offset:0\n"
"v_exp_f32 v200, v200\n"
"v_mul_f32 v201, s56, v190\n"
"v_rcp_f32 v202, v202\n"
"v_add_f32 v200, 1.0, v200\n"
"v_mul_f32 v188, v188, v202\n"
"v_exp_f32 v201, v201\n"
"s_waitcnt lgkmcnt(7)\n"
"v_mfma_f32_16x16x32_bf16 v[168:171], v[152:155], v[80:83], v[168:171]\n"
"ds_read_b64_tr_b16 v[150:151], v254 offset:512\n"
"s_add_u32 s44, s43, 24576\n"
"v_mfma_f32_16x16x32_bf16 v[172:175], v[152:155], v[84:87], v[172:175]\n"
"ds_read_b64_tr_b16 v[152:153], v253 offset:1024\n"
"s_cmp_eq_u32 s44, 122880\n"
"v_mfma_f32_16x16x32_bf16 v[176:179], v[156:159], v[80:83], v[176:179]\n"
"ds_read_b64_tr_b16 v[154:155], v253 offset:1536\n"
"s_cselect_b32 s44, 0, s44\n"
"v_mfma_f32_16x16x32_bf16 v[180:183], v[156:159], v[84:87], v[180:183]\n"
"ds_read_b64_tr_b16 v[156:157], v254 offset:1024\n"
"buffer_load_dword v239, v251, s[36:39], s58 offen\n"
"v_mul_f32 v202, s56, v191\n"
"v_rcp_f32 v200, v200\n"
"v_add_f32 v201, 1.0, v201\n"
"v_mul_f32 v189, v189, v200\n"
"v_cvt_pk_bf16_f32 v212, v188, v189\n"
"s_waitcnt lgkmcnt(7)\n"
"v_mfma_f32_16x16x32_bf16 v[168:171], v[160:163], v[88:91], v[168:171]\n"
"ds_read_b64_tr_b16 v[158:159], v254 offset:1536\n"
"s_add_u32 s58, s58, 2048\n"
"v_mfma_f32_16x16x32_bf16 v[172:175], v[160:163], v[92:95], v[172:175]\n"
"ds_read_b64_tr_b16 v[160:161], v253 offset:2048\n"
"s_cmp_gt_u32 s58, 4096\n"
"v_mfma_f32_16x16x32_bf16 v[176:179], v[164:167], v[88:91], v[176:179]\n"
"ds_read_b64_tr_b16 v[162:163], v253 offset:2560\n"
"s_cselect_b32 s58, 0x40000000, s58\n"
"v_mfma_f32_16x16x32_bf16 v[180:183], v[164:167], v[92:95], v[180:183]\n"
"ds_read_b64_tr_b16 v[164:165], v254 offset:2048\n"
"ds_read_b64_tr_b16 v[166:167], v254 offset:2560\n"
"v_exp_f32 v202, v202\n"
"v_mul_f32 v200, s56, v196\n"
"v_rcp_f32 v201, v201\n"
"v_add_f32 v202, 1.0, v202\n"
"v_mul_f32 v190, v190, v201\n"
"v_exp_f32 v200, v200\n"
"s_waitcnt lgkmcnt(8)\n"
"v_mfma_f32_16x16x32_bf16 v[168:171], v[144:147], v[96:99], v[168:171]\n"
"v_mfma_f32_16x16x32_bf16 v[172:175], v[144:147], v[100:103], v[172:175]\n"
"ds_read_b64_tr_b16 v[144:145], v253 offset:3072\n"
"v_mfma_f32_16x16x32_bf16 v[176:179], v[148:151], v[96:99], v[176:179]\n"
"ds_read_b64_tr_b16 v[146:147], v253 offset:3584\n"
"v_mfma_f32_16x16x32_bf16 v[180:183], v[148:151], v[100:103], v[180:183]\n"
"ds_read_b64_tr_b16 v[148:149], v254 offset:3072\n"
"v_mul_f32 v201, s56, v197\n"
"v_rcp_f32 v202, v202\n"
"v_add_f32 v200, 1.0, v200\n"
"v_mul_f32 v191, v191, v202\n"
"v_cvt_pk_bf16_f32 v213, v190, v191\n"
"s_waitcnt lgkmcnt(7)\n"
"v_mfma_f32_16x16x32_bf16 v[168:171], v[152:155], v[104:107], v[168:171]\n"
"ds_read_b64_tr_b16 v[150:151], v254 offset:3584\n"
"s_cmp_eq_u32 s45, 1\n"
"v_mfma_f32_16x16x32_bf16 v[172:175], v[152:155], v[108:111], v[172:175]\n"
"ds_read_b64_tr_b16 v[152:153], v253 offset:4096\n"
"s_cselect_b32 s60, s35, 0\n"
"v_mfma_f32_16x16x32_bf16 v[176:179], v[156:159], v[104:107], v[176:179]\n"
"ds_read_b64_tr_b16 v[154:155], v253 offset:4608\n"
"v_mfma_f32_16x16x32_bf16 v[180:183], v[156:159], v[108:111], v[180:183]\n"
"ds_read_b64_tr_b16 v[156:157], v254 offset:4096\n"
"v_xor_b32 v252, s60, v252\n"
"v_exp_f32 v201, v201\n"
"v_mul_f32 v202, s56, v198\n"
"v_rcp_f32 v200, v200\n"
"v_add_f32 v201, 1.0, v201\n"
"v_mul_f32 v196, v196, v200\n"
"s_waitcnt lgkmcnt(7)\n"
"v_mfma_f32_16x16x32_bf16 v[168:171], v[160:163], v[112:115], v[168:171]\n"
"ds_read_b64_tr_b16 v[158:159], v254 offset:4608\n"
"s_cmp_eq_u32 s45, s69\n"
"v_mfma_f32_16x16x32_bf16 v[172:175], v[160:163], v[116:119], v[172:175]\n"
"ds_read_b64_tr_b16 v[160:161], v253 offset:5120\n"
"s_cbranch_scc1 O26_%=\n"
"E25_%=:\n"
"v_mfma_f32_16x16x32_bf16 v[176:179], v[164:167], v[112:115], v[176:179]\n"
"ds_read_b64_tr_b16 v[162:163], v253 offset:5632\n"
"v_mfma_f32_16x16x32_bf16 v[180:183], v[164:167], v[116:119], v[180:183]\n"
"ds_read_b64_tr_b16 v[164:165], v254 offset:5120\n"
"ds_read_b64_tr_b16 v[166:167], v254 offset:5632\n"
"v_add_u32 v253, s44, v252\n"
"v_xor_b32 v254, 32, v253\n"
"v_exp_f32 v202, v202\n"
"v_mul_f32 v200, s56, v199\n"
"v_rcp_f32 v201, v201\n"
"v_add_f32 v202, 1.0, v202\n"
"s_waitcnt lgkmcnt(8)\n"
"v_mfma_f32_16x16x32_bf16 v[168:171], v[144:147], v[120:123], v[168:171]\n"
"v_mfma_f32_16x16x32_bf16 v[172:175], v[144:147], v[124:127], v[172:175]\n"
"s_barrier\n"
"ds_read_b64_tr_b16 v[144:145], v253 offset:0\n"
"v_mfma_f32_16x16x32_bf16 v[176:179], v[148:151], v[120:123], v[176:179]\n"
"ds_read_b64_tr_b16 v[146:147], v253 offset:512\n"
"v_mfma_f32_16x16x32_bf16 v[180:183], v[148:151], v[124:127], v[180:183]\n"
"ds_read_b64_tr_b16 v[148:149], v254 offset:0\n"
"v_mul_f32 v197, v197, v201\n"
"v_cvt_pk_bf16_f32 v214, v196, v197\n"
"v_exp_f32 v200, v200\n"
"v_rcp_f32 v202, v202\n"
"v_add_f32 v200, 1.0, v200\n"
"v_mul_f32 v198, v198, v202\n"
"s_waitcnt lgkmcnt(7)\n"
"v_mfma_f32_16x16x32_bf16 v[168:171], v[152:155], v[128:131], v[168:171]\n"
"ds_read_b64_tr_b16 v[150:151], v254 offset:512\n"
"s_add_u32 s42, s44, 24576\n"
"v_mfma_f32_16x16x32_bf16 v[172:175], v[152:155], v[132:135], v[172:175]\n"
"ds_read_b64_tr_b16 v[152:153], v253 offset:1024\n"
"s_cmp_eq_u32 s42, 122880\n"
"v_mfma_f32_16x16x32_bf16 v[176:179], v[156:159], v[128:131], v[176:179]\n"
"ds_read_b64_tr_b16 v[154:155], v253 offset:1536\n"
"s_cselect_b32 s42, 0, s42\n"
"v_mfma_f32_16x16x32_bf16 v[180:183], v[156:159], v[132:135], v[180:183]\n"
"ds_read_b64_tr_b16 v[156:157], v254 offset:1024\n"
"v_rcp_f32 v200, v200\n"
"s_nop 0\n"
"v_mul_f32 v199, v199, v200\n"
"v_cvt_pk_bf16_f32 v215, v198, v199\n"
"buffer_store_dwordx4 v[212:215], v249, s[28:31], s41 offen offset:1024 sc1\n"
"v_add_u32 v249, 0x2000, v249\n"
"s_waitcnt lgkmcnt(7)\n"
"v_mfma_f32_16x16x32_bf16 v[184:187], v[160:163], v[136:139], v[168:171]\n"
"ds_read_b64_tr_b16 v[158:159], v254 offset:1536\n"
"s_sub_u32 s45, s45, 1\n"
"v_mfma_f32_16x16x32_bf16 v[188:191], v[160:163], v[140:143], v[172:175]\n"
"ds_read_b64_tr_b16 v[160:161], v253 offset:2048\n"
"s_cmp_lg_u32 s45, 0\n"
"v_mfma_f32_16x16x32_bf16 v[192:195], v[164:167], v[136:139], v[176:179]\n"
"ds_read_b64_tr_b16 v[162:163], v253 offset:2560\n"
"v_mfma_f32_16x16x32_bf16 v[196:199], v[164:167], v[140:143], v[180:183]\n"
"ds_read_b64_tr_b16 v[164:165], v254 offset:2048\n"
"ds_read_b64_tr_b16 v[166:167], v254 offset:2560\n"
"s_cbranch_scc1 LSH_%=\n"
"s_lshl_b32 s60, s48, 5\n"
"s_add_u32 s60, s60, s47\n"
"s_mul_i32 s60, s60, 9216\n"
"s_add_u32 s60, s60, 65536\n"
"buffer_load_dwordx4 v[176:179], v224, s[28:31], s60 offen sc1\n"
"v_mov_b32 v180, 0\n"
"v_mov_b32 v181, 0\n"
"v_mov_b32 v182, 0\n"
"v_mov_b32 v183, 0\n"
"s_mov_b32 exec_hi, 0\n"
"buffer_load_dwordx4 v[180:183], v226, s[28:31], s60 offen sc1\n"
"s_mov_b32 exec_hi, -1\n"
"s_waitcnt lgkmcnt(10)\n"
"v_mfma_f32_16x16x32_bf16 v[168:171], v[144:147], v[0:3], 0\n"
"v_mfma_f32_16x16x32_bf16 v[172:175], v[144:147], v[4:7], 0\n"
"ds_read_b64_tr_b16 v[144:145], v253 offset:3072\n"
"s_waitcnt lgkmcnt(7)\n"
"v_mfma_f32_16x16x32_bf16 v[168:171], v[152:155], v[8:11], v[168:171]\n"
"ds_read_b64_tr_b16 v[146:147], v253 offset:3584\n"
"v_mfma_f32_16x16x32_bf16 v[172:175], v[152:155], v[12:15], v[172:175]\n"
"ds_read_b64_tr_b16 v[152:153], v253 offset:4096\n"
"v_mul_f32 v200, s56, v184\n"
"v_exp_f32 v200, v200\n"
"v_mul_f32 v201, s56, v185\n"
"v_add_f32 v200, 1.0, v200\n"
"v_exp_f32 v201, v201\n"
"s_waitcnt lgkmcnt(5)\n"
"v_mfma_f32_16x16x32_bf16 v[168:171], v[160:163], v[16:19], v[168:171]\n"
"ds_read_b64_tr_b16 v[154:155], v253 offset:4608\n"
"v_mfma_f32_16x16x32_bf16 v[172:175], v[160:163], v[20:23], v[172:175]\n"
"ds_read_b64_tr_b16 v[160:161], v253 offset:5120\n"
"ds_read_b64_tr_b16 v[162:163], v253 offset:5632\n"
"v_add_u32 v253, s42, v252\n"
"v_mul_f32 v202, s56, v186\n"
"v_rcp_f32 v200, v200\n"
"v_add_f32 v201, 1.0, v201\n"
"v_mul_f32 v184, v184, v200\n"
"v_exp_f32 v202, v202\n"
"v_mul_f32 v200, s56, v187\n"
"s_waitcnt lgkmcnt(4)\n"
"v_mfma_f32_16x16x32_bf16 v[168:171], v[144:147], v[24:27], v[168:171]\n"
"v_mfma_f32_16x16x32_bf16 v[172:175], v[144:147], v[28:31], v[172:175]\n"
"s_barrier\n"
"ds_read_b64_tr_b16 v[144:145], v253 offset:0\n"
"s_add_u32 s43, s42, 24576\n"
"v_rcp_f32 v201, v201\n"
"v_add_f32 v202, 1.0, v202\n"
"v_mul_f32 v185, v185, v201\n"
"v_cvt_pk_bf16_f32 v208, v184, v185\n"
"v_exp_f32 v200, v200\n"
"v_mul_f32 v201, s56, v192\n"
"s_waitcnt lgkmcnt(3)\n"
"v_mfma_f32_16x16x32_bf16 v[168:171], v[152:155], v[32:35], v[168:171]\n"
"ds_read_b64_tr_b16 v[146:147], v253 offset:512\n"
"s_cmp_eq_u32 s43, 122880\n"
"v_mfma_f32_16x16x32_bf16 v[172:175], v[152:155], v[36:39], v[172:175]\n"
"ds_read_b64_tr_b16 v[152:153], v253 offset:1024\n"
"s_cselect_b32 s43, 0, s43\n"
"v_bfe_u32 v241, v255, 2, 7\n"
"v_and_b32 v240, 63, v241\n"
"s_lshl_b32 s60, s47, 9\n"
"s_add_u32 s60, s60, 32768\n"
"s_lshl_b32 s61, s94, 7\n"
"s_add_u32 s61, s61, s60\n"
"buffer_load_dword v245, v241, s[28:31], s61 offen sc1\n"
"buffer_load_dword v246, v240, s[28:31], s60 offen offset:384 sc1\n"
"buffer_load_dword v247, v240, s[28:31], s60 offen offset:448 sc1\n"
"v_rcp_f32 v202, v202\n"
"v_add_f32 v200, 1.0, v200\n"
"v_mul_f32 v186, v186, v202\n"
"v_exp_f32 v201, v201\n"
"v_mul_f32 v202, s56, v193\n"
"v_rcp_f32 v200, v200\n"
"s_waitcnt lgkmcnt(3)\n"
"v_mfma_f32_16x16x32_bf16 v[168:171], v[160:163], v[40:43], v[168:171]\n"
"ds_read_b64_tr_b16 v[154:155], v253 offset:1536\n"
"v_mfma_f32_16x16x32_bf16 v[172:175], v[160:163], v[44:47], v[172:175]\n"
"ds_read_b64_tr_b16 v[160:161], v253 offset:2048\n"
"ds_read_b64_tr_b16 v[162:163], v253 offset:2560\n"
"v_add_f32 v201, 1.0, v201\n"
"v_mul_f32 v187, v187, v200\n"
"v_cvt_pk_bf16_f32 v209, v186, v187\n"
"v_exp_f32 v202, v202\n"
"v_mul_f32 v200, s56, v194\n"
"s_waitcnt lgkmcnt(4)\n"
"v_mfma_f32_16x16x32_bf16 v[168:171], v[144:147], v[48:51], v[168:171]\n"
"v_mfma_f32_16x16x32_bf16 v[172:175], v[144:147], v[52:55], v[172:175]\n"
"ds_read_b64_tr_b16 v[144:145], v253 offset:3072\n"
"v_rcp_f32 v201, v201\n"
"v_add_f32 v202, 1.0, v202\n"
"v_mul_f32 v192, v192, v201\n"
"v_exp_f32 v200, v200\n"
"v_mul_f32 v201, s56, v195\n"
"v_rcp_f32 v202, v202\n"
"s_waitcnt lgkmcnt(3)\n"
"v_mfma_f32_16x16x32_bf16 v[168:171], v[152:155], v[56:59], v[168:171]\n"
"ds_read_b64_tr_b16 v[146:147], v253 offset:3584\n"
"v_mfma_f32_16x16x32_bf16 v[172:175], v[152:155], v[60:63], v[172:175]\n"
"ds_read_b64_tr_b16 v[152:153], v253 offset:4096\n"
"v_add_f32 v200, 1.0, v200\n"
"v_mul_f32 v193, v193, v202\n"
"v_cvt_pk_bf16_f32 v210, v192, v193\n"
"v_exp_f32 v201, v201\n"
"v_mul_f32 v202, s56, v188\n"
"v_rcp_f32 v200, v200\n"
"s_waitcnt lgkmcnt(3)\n"
"v_mfma_f32_16x16x32_bf16 v[168:171], v[160:163], v[64:67], v[168:171]\n"
"ds_read_b64_tr_b16 v[154:155], v253 offset:4608\n"
"v_mfma_f32_16x16x32_bf16 v[172:175], v[160:163], v[68:71], v[172:175]\n"
"ds_read_b64_tr_b16 v[160:161], v253 offset:5120\n"
"ds_read_b64_tr_b16 v[162:163], v253 offset:5632\n"
"v_add_u32 v253, s43, v252\n"
"v_add_f32 v201, 1.0, v201\n"
"v_mul_f32 v194, v194, v200\n"
"v_exp_f32 v202, v202\n"
"v_mul_f32 v200, s56, v189\n"
"v_rcp_f32 v201, v201\n"
"v_add_f32 v202, 1.0, v202\n"
"s_waitcnt lgkmcnt(4)\n"
"v_mfma_f32_16x16x32_bf16 v[168:171], v[144:147], v[72:75], v[168:171]\n"
"v_mfma_f32_16x16x32_bf16 v[172:175], v[144:147], v[76:79], v[172:175]\n"
"s_barrier\n"
"ds_read_b64_tr_b16 v[144:145], v253 offset:0\n"
"v_mul_f32 v195, v195, v201\n"
"v_cvt_pk_bf16_f32 v211, v194, v195\n"
"buffer_store_dwordx4 v[208:211], v249, s[28:31], s41 offen sc1\n"
"v_exp_f32 v200, v200\n"
"v_mul_f32 v201, s56, v190\n"
"s_waitcnt lgkmcnt(3)\n"
"v_mfma_f32_16x16x32_bf16 v[168:171], v[152:155], v[80:83], v[168:171]\n"
"ds_read_b64_tr_b16 v[146:147], v253 offset:512\n"
"v_mfma_f32_16x16x32_bf16 v[172:175], v[152:155], v[84:87], v[172:175]\n"
"ds_read_b64_tr_b16 v[152:153], v253 offset:1024\n"
"v_rcp_f32 v202, v202\n"
"v_add_f32 v200, 1.0, v200\n"
"v_mul_f32 v188, v188, v202\n"
"v_exp_f32 v201, v201\n"
"v_mul_f32 v202, s56, v191\n"
"v_rcp_f32 v200, v200\n"
"s_waitcnt lgkmcnt(3)\n"
"v_mfma_f32_16x16x32_bf16 v[168:171], v[160:163], v[88:91], v[168:171]\n"
"ds_read_b64_tr_b16 v[154:155], v253 offset:1536\n"
"v_mfma_f32_16x16x32_bf16 v[172:175], v[160:163], v[92:95], v[172:175]\n"
"ds_read_b64_tr_b16 v[160:161], v253 offset:2048\n"
"ds_read_b64_tr_b16 v[162:163], v253 offset:2560\n"
"v_add_f32 v201, 1.0, v201\n"
"v_mul_f32 v189, v189, v200\n"
"v_cvt_pk_bf16_f32 v212, v188, v189\n"
"v_exp_f32 v202, v202\n"
"v_mul_f32 v200, s56, v196\n"
"v_rcp_f32 v201, v201\n"
"s_waitcnt lgkmcnt(4)\n"
"v_mfma_f32_16x16x32_bf16 v[168:171], v[144:147], v[96:99], v[168:171]\n"
"v_mfma_f32_16x16x32_bf16 v[172:175], v[144:147], v[100:103], v[172:175]\n"
"ds_read_b64_tr_b16 v[144:145], v253 offset:3072\n"
"v_add_f32 v202, 1.0, v202\n"
"v_mul_f32 v190, v190, v201\n"
"v_exp_f32 v200, v200\n"
"v_mul_f32 v201, s56, v197\n"
"v_rcp_f32 v202, v202\n"
"v_add_f32 v200, 1.0, v200\n"
"s_waitcnt lgkmcnt(3)\n"
"v_mfma_f32_16x16x32_bf16 v[168:171], v[152:155], v[104:107], v[168:171]\n"
"ds_read_b64_tr_b16 v[146:147], v253 offset:3584\n"
"v_mfma_f32_16x16x32_bf16 v[172:175], v[152:155], v[108:111], v[172:175]\n"
"ds_read_b64_tr_b16 v[152:153], v253 offset:4096\n"
"v_mul_f32 v191, v191, v202\n"
"v_cvt_pk_bf16_f32 v213, v190, v191\n"
"v_exp_f32 v201, v201\n"
"v_mul_f32 v202, s56, v198\n"
"v_rcp_f32 v200, v200\n"
"s_waitcnt lgkmcnt(3)\n"
"v_mfma_f32_16x16x32_bf16 v[168:171], v[160:163], v[112:115], v[168:171]\n"
"ds_read_b64_tr_b16 v[154:155], v253 offset:4608\n"
"v_mfma_f32_16x16x32_bf16 v[172:175], v[160:163], v[116:119], v[172:175]\n"
"ds_read_b64_tr_b16 v[160:161], v253 offset:5120\n"
"ds_read_b64_tr_b16 v[162:163], v253 offset:5632\n"
"v_add_f32 v201, 1.0, v201\n"
"v_mul_f32 v196, v196, v200\n"
"v_exp_f32 v202, v202\n"
"v_mul_f32 v200, s56, v199\n"
"v_rcp_f32 v201, v201\n"
"v_add_f32 v202, 1.0, v202\n"
"s_waitcnt lgkmcnt(4)\n"
"v_mfma_f32_16x16x32_bf16 v[168:171], v[144:147], v[120:123], v[168:171]\n"
"v_mfma_f32_16x16x32_bf16 v[172:175], v[144:147], v[124:127], v[172:175]\n"
"s_barrier\n"
"v_mul_f32 v197, v197, v201\n"
"v_cvt_pk_bf16_f32 v214, v196, v197\n"
"v_exp_f32 v200, v200\n"
"v_rcp_f32 v202, v202\n"
"v_add_f32 v200, 1.0, v200\n"
"v_mul_f32 v198, v198, v202\n"
"s_waitcnt lgkmcnt(2)\n"
"v_mfma_f32_16x16x32_bf16 v[168:171], v[152:155], v[128:131], v[168:171]\n"
"v_mfma_f32_16x16x32_bf16 v[172:175], v[152:155], v[132:135], v[172:175]\n"
"v_rcp_f32 v200, v200\n"
"s_nop 0\n"
"v_mul_f32 v199, v199, v200\n"
"v_cvt_pk_bf16_f32 v215, v198, v199\n"
"buffer_store_dwordx4 v[212:215], v249, s[28:31], s41 offen offset:1024 sc1\n"
"v_add_u32 v249, 0x2000, v249\n"
"s_waitcnt lgkmcnt(0)\n"
"v_mfma_f32_16x16x32_bf16 v[168:171], v[160:163], v[136:139], v[168:171]\n"
"v_mfma_f32_16x16x32_bf16 v[172:175], v[160:163], v[140:143], v[172:175]\n"
"s_waitcnt vmcnt(2)\n"
"s_setprio 1\n"
"s_barrier\n"
"ds_read_b128 v[160:163], v224 offset:0\n"
"ds_read_b128 v[164:167], v226 offset:0\n"
"ds_read_b128 v[104:107], v225 offset:0\n"
"ds_read_b128 v[108:111], v227 offset:0\n"
"s_mov_b32 exec_hi, 0\n"
"v_and_b32 v176, s88, v176\n"
"v_and_b32 v177, s89, v177\n"
"v_and_b32 v178, s90, v178\n"
"s_mov_b32 exec_hi, -1\n"
"v_and_b32 v181, s91, v181\n"
"v_and_b32 v182, s92, v182\n"
"v_and_b32 v183, s93, v183\n"
"v_mul_f32 v200, s56, v168\n"
"v_mul_f32 v201, s56, v169\n"
"v_mul_f32 v202, s56, v170\n"
"v_mul_f32 v203, s56, v171\n"
"v_mul_f32 v204, s56, v172\n"
"v_mul_f32 v205, s56, v173\n"
"v_mul_f32 v206, s56, v174\n"
"v_mul_f32 v207, s56, v175\n"
"s_waitcnt lgkmcnt(2)\n"
"v_mfma_f32_32x32x16_bf16 v[184:199], v[160:163], v[176:179], 0\n"
"ds_read_b128 v[112:115], v224 offset:19456\n"
"ds_read_b128 v[116:119], v226 offset:19456\n"
"v_exp_f32 v200, v200\n"
"v_exp_f32 v201, v201\n"
"v_exp_f32 v202, v202\n"
"v_exp_f32 v203, v203\n"
"v_mfma_f32_32x32x16_bf16 v[184:199], v[164:167], v[180:183], v[184:199]\n"
"ds_read_b64_tr_b16 v[136:137], v230 offset:0\n"
"ds_read_b64_tr_b16 v[138:139], v231 offset:0\n"
"ds_read_b64_tr_b16 v[140:141], v230 offset:256\n"
"ds_read_b64_tr_b16 v[142:143], v231 offset:256\n"
"v_exp_f32 v204, v204\n"
"v_exp_f32 v205, v205\n"
"v_exp_f32 v206, v206\n"
"v_exp_f32 v207, v207\n"
"s_and_b32 s60, s49, 1\n"
"s_mul_i32 s60, s60, 3072\n"
"v_add_f32 v200, 1.0, v200\n"
"v_add_f32 v201, 1.0, v201\n"
"v_add_f32 v202, 1.0, v202\n"
"v_add_f32 v203, 1.0, v203\n"
"v_add_f32 v204, 1.0, v204\n"
"v_add_f32 v205, 1.0, v205\n"
"v_add_f32 v206, 1.0, v206\n"
"v_add_f32 v207, 1.0, v207\n"
"v_max3_f32 v240, v184, v185, v186\n"
"v_rcp_f32 v200, v200\n"
"v_max3_f32 v240, v187, v188, v240\n"
"v_rcp_f32 v201, v201\n"
"v_max3_f32 v240, v189, v190, v240\n"
"v_rcp_f32 v202, v202\n"
"v_max3_f32 v240, v191, v192, v240\n"
"v_rcp_f32 v203, v203\n"
"v_max3_f32 v240, v193, v194, v240\n"
"v_rcp_f32 v204, v204\n"
"v_max3_f32 v240, v195, v196, v240\n"
"v_rcp_f32 v205, v205\n"
"v_max3_f32 v240, v197, v198, v240\n"
"v_rcp_f32 v206, v206\n"
"v_max_f32 v240, v199, v240\n"
"v_rcp_f32 v207, v207\n"
"s_mov_b32 exec_lo, 0\n"
"v_cvt_pk_bf16_f32 v180, -v240, 0\n"
"s_mov_b32 exec_lo, -1\n"
"s_waitcnt lgkmcnt(6)\n"
"v_mfma_f32_32x32x16_bf16 v[144:159], v[104:107], v[176:179], 0\n"
"v_subrev_u32 v249, s60, v249\n"
"v_mul_f32 v200, v200, v168\n"
"v_mul_f32 v201, v201, v169\n"
"v_mul_f32 v202, v202, v170\n"
"v_mul_f32 v203, v203, v171\n"
"v_mfma_f32_32x32x16_bf16 v[144:159], v[108:111], v[180:183], v[144:159]\n"
"v_mul_f32 v204, v204, v172\n"
"v_mul_f32 v205, v205, v173\n"
"v_mul_f32 v206, v206, v174\n"
"v_mul_f32 v207, v207, v175\n"
"v_cvt_pk_bf16_f32 v208, v200, v201\n"
"v_cvt_pk_bf16_f32 v209, v202, v203\n"
"v_cvt_pk_bf16_f32 v210, v204, v205\n"
"v_cvt_pk_bf16_f32 v211, v206, v207\n"
"buffer_store_dwordx4 v[208:211], v249, s[28:31], s41 offen sc1\n"
"s_setprio 0\n"
"v_add_u32 v253, 124928, v255\n"
"v_add_u32 v254, 100352, v255\n"
"v_add_u32 v241, 157696, v255\n"
"ds_read_b128 v[48:51], v253 offset:24576\n"
"ds_read_b128 v[52:55], v253 offset:25600\n"
"v_exp_f32 v144, v144\n"
"v_exp_f32 v145, v145\n"
"ds_read_b64_tr_b16 v[104:105], v228 offset:19456\n"
"ds_read_b64_tr_b16 v[106:107], v229 offset:19456\n"
"ds_read_b64_tr_b16 v[108:109], v228 offset:19712\n"
"ds_read_b64_tr_b16 v[110:111], v229 offset:19712\n"
"v_exp_f32 v146, v146\n"
"v_exp_f32 v147, v147\n"
"v_cvt_pk_bf16_f32 v144, v144, v145\n"
"v_cvt_pk_bf16_f32 v145, v146, v147\n"
"s_waitcnt lgkmcnt(10)\n"
"v_mfma_f32_32x32x16_bf16 v[184:199], v[112:115], v[176:179], 0\n"
"ds_read_b128 v[120:123], v225 offset:19456\n"
"ds_read_b128 v[124:127], v227 offset:19456\n"
"ds_read_b128 v[56:59], v253 offset:28672\n"
"ds_read_b128 v[60:63], v253 offset:29696\n"
"v_exp_f32 v148, v148\n"
"v_exp_f32 v149, v149\n"
"v_exp_f32 v150, v150\n"
"v_exp_f32 v151, v151\n"
"v_cvt_pk_bf16_f32 v146, v148, v149\n"
"v_cvt_pk_bf16_f32 v147, v150, v151\n"
"v_mfma_f32_32x32x16_bf16 v[184:199], v[116:119], v[180:183], v[184:199]\n"
"ds_read_b64_tr_b16 v[168:169], v228 offset:0\n"
"ds_read_b64_tr_b16 v[170:171], v229 offset:0\n"
"ds_read_b64_tr_b16 v[172:173], v228 offset:256\n"
"ds_read_b64_tr_b16 v[174:175], v229 offset:256\n"
"v_exp_f32 v152, v152\n"
"v_exp_f32 v153, v153\n"
"v_exp_f32 v154, v154\n"
"v_exp_f32 v155, v155\n"
"s_waitcnt lgkmcnt(14)\n"
"v_mfma_f32_32x32x16_bf16 v[200:215], v[136:139], v[144:147], 0\n"
"v_cvt_pk_bf16_f32 v148, v152, v153\n"
"v_cvt_pk_bf16_f32 v149, v154, v155\n"
"ds_read_b128 v[16:19], v253 offset:8192\n"
"ds_read_b128 v[20:23], v253 offset:9216\n"
"v_exp_f32 v156, v156\n"
"v_exp_f32 v157, v157\n"
"v_exp_f32 v158, v158\n"
"v_exp_f32 v159, v159\n"
"v_cvt_pk_bf16_f32 v150, v156, v157\n"
"v_cvt_pk_bf16_f32 v151, v158, v159\n"
"v_exp_f32 v184, v184\n"
"v_exp_f32 v185, v185\n"
"v_mfma_f32_32x32x16_bf16 v[200:215], v[140:143], v[148:151], v[200:215]\n"
"ds_read_b128 v[128:131], v224 offset:38912\n"
"ds_read_b128 v[132:135], v226 offset:38912\n"
"ds_read_b128 v[24:27], v253 offset:12288\n"
"ds_read_b128 v[28:31], v253 offset:13312\n"
"v_exp_f32 v186, v186\n"
"v_exp_f32 v187, v187\n"
"v_cvt_pk_bf16_f32 v184, v184, v185\n"
"v_cvt_pk_bf16_f32 v185, v186, v187\n"
"s_waitcnt lgkmcnt(12)\n"
"v_mfma_f32_32x32x16_bf16 v[144:159], v[120:123], v[176:179], 0\n"
"ds_read_b64_tr_b16 v[112:113], v230 offset:19456\n"
"ds_read_b64_tr_b16 v[114:115], v231 offset:19456\n"
"ds_read_b64_tr_b16 v[116:117], v230 offset:19712\n"
"ds_read_b64_tr_b16 v[118:119], v231 offset:19712\n"
"s_waitcnt vmcnt(0)\n"
"v_mov_b32 v242, s50\n"
"v_mov_b32 v243, s51\n"
"s_mov_b64 exec, 1\n"
"buffer_store_dwordx2 v[242:243], off, s[28:31], s100 offset:8 sc1\n"
"s_mov_b64 exec, -1\n"
"v_exp_f32 v188, v188\n"
"v_exp_f32 v189, v189\n"
"v_exp_f32 v190, v190\n"
"v_exp_f32 v191, v191\n"
"v_cvt_pk_bf16_f32 v186, v188, v189\n"
"v_cvt_pk_bf16_f32 v187, v190, v191\n"
"v_mfma_f32_32x32x16_bf16 v[144:159], v[124:127], v[180:183], v[144:159]\n"
"ds_read_b64_tr_b16 v[80:81], v228 offset:38912\n"
"ds_read_b64_tr_b16 v[82:83], v229 offset:38912\n"
"ds_read_b64_tr_b16 v[84:85], v228 offset:39168\n"
"ds_read_b64_tr_b16 v[86:87], v229 offset:39168\n"
"v_exp_f32 v192, v192\n"
"v_exp_f32 v193, v193\n"
"v_exp_f32 v194, v194\n"
"v_exp_f32 v195, v195\n"
"s_waitcnt lgkmcnt(15)\n"
"v_mfma_f32_32x32x16_bf16 v[200:215], v[104:107], v[184:187], v[200:215]\n"
"v_cvt_pk_bf16_f32 v188, v192, v193\n"
"v_cvt_pk_bf16_f32 v189, v194, v195\n"
"ds_read_b128 v[0:3], v253 offset:0\n"
"ds_read_b128 v[4:7], v253 offset:1024\n"
"ds_read_b128 v[8:11], v253 offset:4096\n"
"ds_read_b128 v[12:15], v253 offset:5120\n"
"s_mov_b64 s[36:37], s[8:9]\n"
"s_mov_b32 s38, 663552\n"
"s_mul_i32 s66, s48, 82944\n"
"s_lshl_b32 s67, s49, 7\n"
"s_add_u32 s67, s67, s66\n"
"s_add_u32 s66, s66, 1024\n"
"s_add_u32 m0, s59, 147456\n"
"v_exp_f32 v196, v196\n"
"v_exp_f32 v197, v197\n"
"v_exp_f32 v198, v198\n"
"v_exp_f32 v199, v199\n"
"v_cvt_pk_bf16_f32 v190, v196, v197\n"
"v_cvt_pk_bf16_f32 v191, v198, v199\n"
"s_waitcnt lgkmcnt(15)\n"
"s_barrier\n"
"v_exp_f32 v144, v144\n"
"v_exp_f32 v145, v145\n"
"v_mfma_f32_32x32x16_bf16 v[200:215], v[108:111], v[188:191], v[200:215]\n"
"ds_read_b128 v[72:75], v225 offset:38912\n"
"ds_read_b128 v[76:79], v227 offset:38912\n"
"ds_read_b128 v[120:123], v224 offset:58368\n"
"ds_read_b128 v[124:127], v226 offset:58368\n"
"buffer_load_dwordx4 v244, s[36:39], s67 offen lds\n"
"s_add_u32 m0, m0, 4096\n"
"s_add_u32 s67, s67, 512\n"
"v_exp_f32 v146, v146\n"
"v_exp_f32 v147, v147\n"
"v_cvt_pk_bf16_f32 v144, v144, v145\n"
"v_cvt_pk_bf16_f32 v145, v146, v147\n"
"s_waitcnt lgkmcnt(15)\n"
"v_mfma_f32_32x32x16_bf16 v[184:199], v[128:131], v[176:179], 0\n"
"ds_read_b64_tr_b16 v[88:89], v230 offset:38912\n"
"ds_read_b64_tr_b16 v[90:91], v231 offset:38912\n"
"ds_read_b64_tr_b16 v[92:93], v230 offset:39168\n"
"ds_read_b64_tr_b16 v[94:95], v231 offset:39168\n"
"buffer_load_dwordx4 v244, s[36:39], s67 offen lds\n"
"s_mov_b32 m0, 159744\n"
"v_exp_f32 v148, v148\n"
"v_exp_f32 v149, v149\n"
"v_exp_f32 v150, v150\n"
"v_exp_f32 v151, v151\n"
"v_cvt_pk_bf16_f32 v146, v148, v149\n"
"v_cvt_pk_bf16_f32 v147, v150, v151\n"
"v_mfma_f32_32x32x16_bf16 v[184:199], v[132:135], v[180:183], v[184:199]\n"
"ds_read_b64_tr_b16 v[96:97], v228 offset:58368\n"
"ds_read_b64_tr_b16 v[98:99], v229 offset:58368\n"
"ds_read_b64_tr_b16 v[100:101], v228 offset:58624\n"
"ds_read_b64_tr_b16 v[102:103], v229 offset:58624\n"
"v_add_u32 v228, 19456, v228\n"
"v_add_u32 v229, 19456, v229\n"
"buffer_load_dwordx4 v244, s[36:39], s66 offen lds\n"
"v_exp_f32 v152, v152\n"
"v_exp_f32 v153, v153\n"
"v_exp_f32 v154, v154\n"
"v_exp_f32 v155, v155\n"
"s_waitcnt lgkmcnt(15)\n"
"v_mfma_f32_32x32x16_bf16 v[200:215], v[112:115], v[144:147], v[200:215]\n"
"v_cvt_pk_bf16_f32 v148, v152, v153\n"
"v_cvt_pk_bf16_f32 v149, v154, v155\n"
"ds_read_b64_tr_b16 v[136:137], v228 offset:58368\n"
"ds_read_b64_tr_b16 v[138:139], v229 offset:58368\n"
"ds_read_b64_tr_b16 v[140:141], v228 offset:58624\n"
"ds_read_b64_tr_b16 v[142:143], v229 offset:58624\n"
"ds_read_b128 v[32:35], v253 offset:16384\n"
"ds_read_b128 v[36:39], v253 offset:17408\n"
"v_exp_f32 v156, v156\n"
"v_exp_f32 v157, v157\n"
"v_exp_f32 v158, v158\n"
"v_exp_f32 v159, v159\n"
"v_cvt_pk_bf16_f32 v150, v156, v157\n"
"v_cvt_pk_bf16_f32 v151, v158, v159\n"
"v_exp_f32 v184, v184\n"
"v_exp_f32 v185, v185\n"
"v_mfma_f32_32x32x16_bf16 v[200:215], v[116:119], v[148:151], v[200:215]\n"
"v_add_u32 v224, 19456, v224\n"
"v_add_u32 v226, 19456, v226\n"
"ds_read_b64_tr_b16 v[104:105], v230 offset:58368\n"
"ds_read_b64_tr_b16 v[106:107], v231 offset:58368\n"
"ds_read_b64_tr_b16 v[108:109], v230 offset:58624\n"
"ds_read_b64_tr_b16 v[110:111], v231 offset:58624\n"
"v_exp_f32 v186, v186\n"
"v_exp_f32 v187, v187\n"
"v_cvt_pk_bf16_f32 v184, v184, v185\n"
"v_cvt_pk_bf16_f32 v185, v186, v187\n"
"s_waitcnt lgkmcnt(15)\n"
"v_mfma_f32_32x32x16_bf16 v[144:159], v[72:75], v[176:179], 0\n"
"ds_read_b128 v[128:131], v225 offset:58368\n"
"ds_read_b128 v[132:135], v227 offset:58368\n"
"ds_read_b128 v[112:115], v224 offset:58368\n"
"ds_read_b128 v[116:119], v226 offset:58368\n"
"v_exp_f32 v188, v188\n"
"v_exp_f32 v189, v189\n"
"v_exp_f32 v190, v190\n"
"v_exp_f32 v191, v191\n"
"v_cvt_pk_bf16_f32 v186, v188, v189\n"
"v_cvt_pk_bf16_f32 v187, v190, v191\n"
"v_mfma_f32_32x32x16_bf16 v[144:159], v[76:79], v[180:183], v[144:159]\n"
"ds_read_b128 v[40:43], v253 offset:20480\n"
"ds_read_b128 v[44:47], v253 offset:21504\n"
"ds_read2_b64 v[64:67], v241 offset0:0 offset1:128\n"
"ds_read2_b64 v[68:71], v241 offset0:1 offset1:129\n"
"v_exp_f32 v192, v192\n"
"v_exp_f32 v193, v193\n"
"v_exp_f32 v194, v194\n"
"v_exp_f32 v195, v195\n"
"s_waitcnt lgkmcnt(15)\n"
"v_mfma_f32_32x32x16_bf16 v[200:215], v[80:83], v[184:187], v[200:215]\n"
"v_cvt_pk_bf16_f32 v188, v192, v193\n"
"v_cvt_pk_bf16_f32 v189, v194, v195\n"
"v_exp_f32 v196, v196\n"
"v_exp_f32 v197, v197\n"
"v_exp_f32 v198, v198\n"
"v_exp_f32 v199, v199\n"
"v_cvt_pk_bf16_f32 v190, v196, v197\n"
"v_cvt_pk_bf16_f32 v191, v198, v199\n"
"s_waitcnt lgkmcnt(0)\n"
"s_barrier\n"
"v_exp_f32 v144, v144\n"
"v_exp_f32 v145, v145\n"
"v_mfma_f32_32x32x16_bf16 v[200:215], v[84:87], v[188:191], v[200:215]\n"
"ds_read_b128 v[72:75], v254 offset:0\n"
"ds_read_b128 v[76:79], v254 offset:1024\n"
"v_exp_f32 v146, v146\n"
"v_exp_f32 v147, v147\n"
"v_cvt_pk_bf16_f32 v144, v144, v145\n"
"v_cvt_pk_bf16_f32 v145, v146, v147\n"
"s_waitcnt lgkmcnt(15)\n"
"v_mfma_f32_32x32x16_bf16 v[184:199], v[120:123], v[176:179], 0\n"
"ds_read_b128 v[80:83], v254 offset:4096\n"
"ds_read_b128 v[84:87], v254 offset:5120\n"
"v_exp_f32 v148, v148\n"
"v_exp_f32 v149, v149\n"
"v_exp_f32 v150, v150\n"
"v_exp_f32 v151, v151\n"
"v_cvt_pk_bf16_f32 v146, v148, v149\n"
"v_cvt_pk_bf16_f32 v147, v150, v151\n"
"v_mfma_f32_32x32x16_bf16 v[184:199], v[124:127], v[180:183], v[184:199]\n"
"v_exp_f32 v152, v152\n"
"v_exp_f32 v153, v153\n"
"v_exp_f32 v154, v154\n"
"v_exp_f32 v155, v155\n"
"s_waitcnt lgkmcnt(15)\n"
"v_mfma_f32_32x32x16_bf16 v[200:215], v[88:91], v[144:147], v[200:215]\n"
"v_cvt_pk_bf16_f32 v148, v152, v153\n"
"v_cvt_pk_bf16_f32 v149, v154, v155\n"
"v_exp_f32 v156, v156\n"
"v_exp_f32 v157, v157\n"
"v_exp_f32 v158, v158\n"
"v_exp_f32 v159, v159\n"
"v_cvt_pk_bf16_f32 v150, v156, v157\n"
"v_cvt_pk_bf16_f32 v151, v158, v159\n"
"v_exp_f32 v184, v184\n"
"v_exp_f32 v185, v185\n"
"v_mfma_f32_32x32x16_bf16 v[200:215], v[92:95], v[148:151], v[200:215]\n"
"v_exp_f32 v186, v186\n"
"v_exp_f32 v187, v187\n"
"v_cvt_pk_bf16_f32 v184, v184, v185\n"
"v_cvt_pk_bf16_f32 v185, v186, v187\n"
"s_waitcnt lgkmcnt(10)\n"
"v_mfma_f32_32x32x16_bf16 v[144:159], v[128:131], v[176:179], 0\n"
"ds_read_b128 v[88:91], v254 offset:8192\n"
"ds_read_b128 v[92:95], v254 offset:9216\n"
"v_exp_f32 v188, v188\n"
"v_exp_f32 v189, v189\n"
"v_exp_f32 v190, v190\n"
"v_exp_f32 v191, v191\n"
"v_cvt_pk_bf16_f32 v186, v188, v189\n"
"v_cvt_pk_bf16_f32 v187, v190, v191\n"
"v_mfma_f32_32x32x16_bf16 v[144:159], v[132:135], v[180:183], v[144:159]\n"
"v_exp_f32 v192, v192\n"
"v_exp_f32 v193, v193\n"
"v_exp_f32 v194, v194\n"
"v_exp_f32 v195, v195\n"
"s_waitcnt lgkmcnt(15)\n"
"v_mfma_f32_32x32x16_bf16 v[200:215], v[96:99], v[184:187], v[200:215]\n"
"v_cvt_pk_bf16_f32 v188, v192, v193\n"
"v_cvt_pk_bf16_f32 v189, v194, v195\n"
"v_exp_f32 v196, v196\n"
"v_exp_f32 v197, v197\n"
"v_exp_f32 v198, v198\n"
"v_exp_f32 v199, v199\n"
"v_cvt_pk_bf16_f32 v190, v196, v197\n"
"v_cvt_pk_bf16_f32 v191, v198, v199\n"
"v_exp_f32 v144, v144\n"
"v_exp_f32 v145, v145\n"
"v_mfma_f32_32x32x16_bf16 v[200:215], v[100:103], v[188:191], v[200:215]\n"
"v_exp_f32 v146, v146\n"
"v_exp_f32 v147, v147\n"
"v_cvt_pk_bf16_f32 v144, v144, v145\n"
"v_cvt_pk_bf16_f32 v145, v146, v147\n"
"s_waitcnt lgkmcnt(15)\n"
"v_mfma_f32_32x32x16_bf16 v[184:199], v[160:163], v[176:179], 0\n"
"ds_read_b128 v[96:99], v254 offset:12288\n"
"ds_read_b128 v[100:103], v254 offset:13312\n"
"v_exp_f32 v148, v148\n"
"v_exp_f32 v149, v149\n"
"v_exp_f32 v150, v150\n"
"v_exp_f32 v151, v151\n"
"s_waitcnt lgkmcnt(12)\n"
"v_mfma_f32_32x32x16_bf16 v[120:135], v[112:115], v[176:179], 0\n"
"v_cvt_pk_bf16_f32 v146, v148, v149\n"
"v_cvt_pk_bf16_f32 v147, v150, v151\n"
"v_mfma_f32_32x32x16_bf16 v[184:199], v[164:167], v[180:183], v[184:199]\n"
"v_exp_f32 v152, v152\n"
"v_exp_f32 v153, v153\n"
"v_exp_f32 v154, v154\n"
"v_exp_f32 v155, v155\n"
"s_waitcnt lgkmcnt(15)\n"
"v_mfma_f32_32x32x16_bf16 v[200:215], v[104:107], v[144:147], v[200:215]\n"
"v_cvt_pk_bf16_f32 v148, v152, v153\n"
"v_cvt_pk_bf16_f32 v149, v154, v155\n"
"v_mfma_f32_32x32x16_bf16 v[120:135], v[116:119], v[180:183], v[120:135]\n"
"v_exp_f32 v156, v156\n"
"v_exp_f32 v157, v157\n"
"v_exp_f32 v158, v158\n"
"v_exp_f32 v159, v159\n"
"v_cvt_pk_bf16_f32 v150, v156, v157\n"
"v_cvt_pk_bf16_f32 v151, v158, v159\n"
"s_nop 1\n"
"v_mfma_f32_32x32x16_bf16 v[200:215], v[108:111], v[148:151], v[200:215]\n"
"s_mov_b32 s42, 0x8000\n"
"s_lshl_b32 vcc_lo, -1, 0\n"
"s_lshl_b32 vcc_hi, -1, 4\n"
"v_cndmask_b32 v184, v120, v184, vcc\n"
"s_lshl_b32 vcc_lo, -1, 1\n"
"s_lshl_b32 vcc_hi, -1, 5\n"
"v_cndmask_b32 v185, v121, v185, vcc\n"
"v_exp_f32 v184, v184\n"
"s_lshl_b32 vcc_lo, -1, 2\n"
"s_lshl_b32 vcc_hi, -1, 6\n"
"v_cndmask_b32 v186, v122, v186, vcc\n"
"v_exp_f32 v185, v185\n"
"s_lshl_b32 vcc_lo, -1, 3\n"
"s_lshl_b32 vcc_hi, -1, 7\n"
"v_cndmask_b32 v187, v123, v187, vcc\n"
"v_exp_f32 v186, v186\n"
"s_lshl_b32 vcc_lo, -1, 8\n"
"s_lshl_b32 vcc_hi, -1, 12\n"
"v_cndmask_b32 v188, v124, v188, vcc\n"
"v_exp_f32 v187, v187\n"
"v_cvt_pk_bf16_f32 v184, v184, v185\n"
"v_cvt_pk_bf16_f32 v185, v186, v187\n"
"v_and_b32 v216, v216, v184\n"
"v_and_b32 v217, v217, v185\n"
"v_xor_b32 v184, v216, v184\n"
"v_xor_b32 v185, v217, v185\n"
"s_waitcnt vmcnt(0)\n"
"s_waitcnt lgkmcnt(0)\n"
"s_barrier\n"
"ds_read_b64_tr_b16 v[164:165], v234 offset:0\n"
"ds_read_b64_tr_b16 v[166:167], v234 offset:14336\n"
"ds_read_b64_tr_b16 v[144:145], v234 offset:1024\n"
"ds_read_b64_tr_b16 v[146:147], v234 offset:15360\n"
"s_lshl_b32 vcc_lo, -1, 9\n"
"s_lshl_b32 vcc_hi, -1, 13\n"
"v_cndmask_b32 v189, v125, v189, vcc\n"
"v_exp_f32 v188, v188\n"
"s_lshl_b32 vcc_lo, -1, 10\n"
"s_lshl_b32 vcc_hi, -1, 14\n"
"v_cndmask_b32 v190, v126, v190, vcc\n"
"v_exp_f32 v189, v189\n"
"s_lshl_b32 vcc_lo, -1, 11\n"
"s_lshl_b32 vcc_hi, -1, 15\n"
"v_cndmask_b32 v191, v127, v191, vcc\n"
"v_exp_f32 v190, v190\n"
"s_lshl_b32 vcc_lo, -1, 16\n"
"s_lshl_b32 vcc_hi, -1, 20\n"
"v_cndmask_b32 v192, v128, v192, vcc\n"
"v_exp_f32 v191, v191\n"
"v_cvt_pk_bf16_f32 v186, v188, v189\n"
"v_cvt_pk_bf16_f32 v187, v190, v191\n"
"v_and_b32 v218, v218, v186\n"
"v_and_b32 v219, v219, v187\n"
"v_xor_b32 v186, v218, v186\n"
"v_xor_b32 v187, v219, v187\n"
"ds_read_b64_tr_b16 v[148:149], v234 offset:2048\n"
"ds_read_b64_tr_b16 v[150:151], v234 offset:16384\n"
"ds_read_b64_tr_b16 v[152:153], v234 offset:3072\n"
"ds_read_b64_tr_b16 v[154:155], v234 offset:17408\n"
"s_lshl_b32 vcc_lo, -1, 17\n"
"s_lshl_b32 vcc_hi, -1, 21\n"
"v_cndmask_b32 v193, v129, v193, vcc\n"
"v_exp_f32 v192, v192\n"
"s_lshl_b32 vcc_lo, -1, 18\n"
"s_lshl_b32 vcc_hi, -1, 22\n"
"v_cndmask_b32 v194, v130, v194, vcc\n"
"v_exp_f32 v193, v193\n"
"s_lshl_b32 vcc_lo, -1, 19\n"
"s_lshl_b32 vcc_hi, -1, 23\n"
"v_cndmask_b32 v195, v131, v195, vcc\n"
"v_exp_f32 v194, v194\n"
"s_lshl_b32 vcc_lo, -1, 24\n"
"s_lshl_b32 vcc_hi, -1, 28\n"
"v_cndmask_b32 v196, v132, v196, vcc\n"
"v_exp_f32 v195, v195\n"
"s_waitcnt lgkmcnt(15)\n"
"v_mfma_f32_32x32x16_bf16 v[200:215], v[168:171], v[216:219], v[200:215]\n"
"v_cvt_pk_bf16_f32 v188, v192, v193\n"
"v_cvt_pk_bf16_f32 v189, v194, v195\n"
"v_and_b32 v220, v220, v188\n"
"v_and_b32 v221, v221, v189\n"
"v_xor_b32 v188, v220, v188\n"
"v_xor_b32 v189, v221, v189\n"
"v_mfma_f32_32x32x16_bf16 v[200:215], v[136:139], v[184:187], v[200:215]\n"
"ds_read_b64_tr_b16 v[156:157], v234 offset:4096\n"
"ds_read_b64_tr_b16 v[158:159], v234 offset:18432\n"
"ds_read_b64_tr_b16 v[160:161], v234 offset:5120\n"
"ds_read_b64_tr_b16 v[162:163], v234 offset:19456\n"
"s_lshl_b32 vcc_lo, -1, 25\n"
"s_lshl_b32 vcc_hi, -1, 29\n"
"v_cndmask_b32 v197, v133, v197, vcc\n"
"v_exp_f32 v196, v196\n"
"s_lshl_b32 vcc_lo, -1, 26\n"
"s_lshl_b32 vcc_hi, -1, 30\n"
"v_cndmask_b32 v198, v134, v198, vcc\n"
"v_exp_f32 v197, v197\n"
"s_lshl_b32 vcc_lo, -1, 27\n"
"s_lshl_b32 vcc_hi, -1, 31\n"
"v_cndmask_b32 v199, v135, v199, vcc\n"
"v_exp_f32 v198, v198\n"
"v_exp_f32 v199, v199\n"
"v_cvt_pk_bf16_f32 v190, v196, v197\n"
"v_cvt_pk_bf16_f32 v191, v198, v199\n"
"v_and_b32 v222, v222, v190\n"
"v_and_b32 v223, v223, v191\n"
"v_xor_b32 v190, v222, v190\n"
"v_xor_b32 v191, v223, v191\n"
"v_mfma_f32_32x32x16_bf16 v[200:215], v[172:175], v[220:223], v[200:215]\n"
"s_nop 0\n"
"v_mfma_f32_32x32x16_bf16 v[200:215], v[140:143], v[188:191], v[200:215]\n"
"v_and_b32 v228, 0x3ff, v234\n"
"v_add_u32 v228, 147456, v228\n"
"v_add_u32 v230, 129024, v255\n"
"v_add_u32 v241, 16384, v230\n"
"v_add_u32 v242, 30720, v255\n"
"v_lshrrev_b32 v243, 1, v255\n"
"v_add_u32 v243, 95232, v243\n"
"s_mov_b32 s43, 8\n"
"s_mov_b32 s45, 1\n"
"s_mul_i32 s55, s47, 589824\n"
"s_lshl_b32 s60, s48, 13\n"
"s_add_u32 s55, s55, s60\n"
"s_lshl_b32 s60, s46, 10\n"
"s_add_u32 s55, s55, s60\n"
"s_add_u32 s55, s55, 16842752\n"
"v_subrev_u32 v240, 0x2b800000, v212\n"
"v_cmp_lt_u32 vcc, 0x28000000, v240\n"
"s_and_b32 s62, s95, 1\n"
"s_sub_u32 s62, 0, s62\n"
"s_mov_b32 s63, 0\n"
"s_cbranch_vccnz LFB_%=\n"
"LFRH_%=:\n"
"v_rcp_f32 v240, v212\n"
"v_cndmask_b32 v200, v200, v210, s[62:63]\n"
"v_cndmask_b32 v201, v201, v211, s[62:63]\n"
"v_mul_f32 v240, v240, v245\n"
"ds_read_b64_tr_b16 v[188:189], v234 offset:6144\n"
"ds_read_b64_tr_b16 v[190:191], v234 offset:20480\n"
"v_add_u32 v253, s42, v234\n"
"s_waitcnt lgkmcnt(10)\n"
"v_mfma_f32_16x16x32_bf16 v[192:195], v[164:167], v[0:3], 0\n"
"v_mfma_f32_16x16x32_bf16 v[196:199], v[164:167], v[4:7], 0\n"
"ds_read_b64_tr_b16 v[164:165], v253 offset:0\n"
"v_mul_f32 v200, v200, v240\n"
"v_mul_f32 v201, v201, v240\n"
"v_mul_f32 v202, v202, v240\n"
"v_mul_f32 v203, v203, v240\n"
"v_mul_f32 v204, v204, v240\n"
"v_mfma_f32_16x16x32_bf16 v[192:195], v[144:147], v[8:11], v[192:195]\n"
"ds_read_b64_tr_b16 v[166:167], v253 offset:14336\n"
"v_mfma_f32_16x16x32_bf16 v[196:199], v[144:147], v[12:15], v[196:199]\n"
"ds_read_b64_tr_b16 v[168:169], v253 offset:1024\n"
"s_add_u32 s42, s42, 0x8000\n"
"s_and_b32 s42, s42, 0x1ffff\n"
"v_add_u32 v254, s42, v234\n"
"v_mul_f32 v205, v205, v240\n"
"v_mul_f32 v206, v206, v240\n"
"v_mul_f32 v207, v207, v240\n"
"v_mul_f32 v208, v208, v240\n"
"v_mul_f32 v209, v209, v240\n"
"s_waitcnt lgkmcnt(9)\n"
"v_mfma_f32_16x16x32_bf16 v[192:195], v[148:151], v[16:19], v[192:195]\n"
"ds_read_b64_tr_b16 v[170:171], v253 offset:15360\n"
"v_mfma_f32_16x16x32_bf16 v[196:199], v[148:151], v[20:23], v[196:199]\n"
"ds_read_b64_tr_b16 v[172:173], v253 offset:2048\n"
"v_cvt_pk_bf16_f32 v216, v200, v201\n"
"v_cvt_pk_bf16_f32 v217, v202, v203\n"
"v_cvt_pk_bf16_f32 v218, v204, v205\n"
"v_cvt_pk_bf16_f32 v219, v206, v207\n"
"v_mfma_f32_16x16x32_bf16 v[192:195], v[152:155], v[24:27], v[192:195]\n"
"ds_read_b64_tr_b16 v[174:175], v253 offset:16384\n"
"v_mfma_f32_16x16x32_bf16 v[196:199], v[152:155], v[28:31], v[196:199]\n"
"ds_read_b64_tr_b16 v[176:177], v253 offset:3072\n"
"v_cvt_pk_bf16_f32 v220, v208, v209\n"
"ds_write_b128 v232, v[216:219]\n"
"s_mov_b32 exec_lo, 0\n"
"v_mov_b32 v220, 0\n"
"s_mov_b32 exec_lo, -1\n"
"ds_write_b32 v233, v220\n"
"s_waitcnt lgkmcnt(11)\n"
"v_mfma_f32_16x16x32_bf16 v[192:195], v[156:159], v[32:35], v[192:195]\n"
"ds_read_b64_tr_b16 v[178:179], v253 offset:17408\n"
"v_mfma_f32_16x16x32_bf16 v[196:199], v[156:159], v[36:39], v[196:199]\n"
"ds_read_b64_tr_b16 v[180:181], v253 offset:4096\n"
"s_mov_b32 s52, s53\n"
"s_mov_b32 s53, s43\n"
"v_add_u32 v229, 12288, v228\n"
"s_add_u32 s43, s48, s45\n"
"s_and_b32 s43, s43, 7\n"
"v_mfma_f32_16x16x32_bf16 v[192:195], v[160:163], v[40:43], v[192:195]\n"
"ds_read_b64_tr_b16 v[182:183], v253 offset:18432\n"
"v_mfma_f32_16x16x32_bf16 v[196:199], v[160:163], v[44:47], v[196:199]\n"
"ds_read_b64_tr_b16 v[184:185], v253 offset:5120\n"
"s_waitcnt lgkmcnt(11)\n"
"v_mfma_f32_16x16x32_bf16 v[192:195], v[188:191], v[48:51], v[192:195]\n"
"ds_read_b64_tr_b16 v[186:187], v253 offset:19456\n"
"v_mfma_f32_16x16x32_bf16 v[196:199], v[188:191], v[52:55], v[196:199]\n"
"ds_read_b64_tr_b16 v[188:189], v253 offset:6144\n"
"ds_read_b64_tr_b16 v[190:191], v253 offset:20480\n"
"v_mfma_f32_16x16x32_bf16 v[192:195], v[164:167], v[56:59], v[192:195]\n"
"v_mfma_f32_16x16x32_bf16 v[196:199], v[164:167], v[60:63], v[196:199]\n"
"s_waitcnt lgkmcnt(4)\n"
"s_barrier\n"
"ds_read_b64_tr_b16 v[164:165], v254 offset:0\n"
"ds_read_b128 v[104:107], v230 offset:0\n"
"ds_read_b128 v[108:111], v230 offset:1024\n"
"ds_read_b128 v[144:147], v242 offset:0\n"
"ds_read_b128 v[148:151], v242 offset:1024\n"
"s_waitcnt lgkmcnt(15)\n"
"v_mfma_f32_16x16x32_bf16 v[192:195], v[168:171], v[64:67], v[192:195]\n"
"ds_read_b64_tr_b16 v[166:167], v254 offset:14336\n"
"v_mfma_f32_16x16x32_bf16 v[196:199], v[168:171], v[68:71], v[196:199]\n"
"ds_read_b64_tr_b16 v[168:169], v254 offset:1024\n"
"s_add_u32 s42, s42, 0x8000\n"
"s_and_b32 s42, s42, 0x1ffff\n"
"v_add_u32 v252, s42, v234\n"
"ds_read_b128 v[152:155], v242 offset:32768\n"
"ds_read_b128 v[156:159], v242 offset:33792\n"
"v_mfma_f32_16x16x32_bf16 v[192:195], v[172:175], v[72:75], v[192:195]\n"
"ds_read_b64_tr_b16 v[170:171], v254 offset:15360\n"
"v_mfma_f32_16x16x32_bf16 v[196:199], v[172:175], v[76:79], v[196:199]\n"
"ds_read_b64_tr_b16 v[172:173], v229\n"
"ds_read_b64 v[160:161], v243 offset:0\n"
"ds_read_b64 v[162:163], v243 offset:512\n"
"s_waitcnt lgkmcnt(15)\n"
"v_mfma_f32_16x16x32_bf16 v[192:195], v[176:179], v[80:83], v[192:195]\n"
"v_mfma_f32_16x16x32_bf16 v[196:199], v[176:179], v[84:87], v[196:199]\n"
"ds_read_b64_tr_b16 v[176:177], v254 offset:3072\n"
"ds_read_b128 v[112:115], v230 offset:4096\n"
"ds_read_b128 v[116:119], v230 offset:5120\n"
"v_mfma_f32_16x16x32_bf16 v[192:195], v[180:183], v[88:91], v[192:195]\n"
"ds_read_b64_tr_b16 v[178:179], v254 offset:17408\n"
"v_mfma_f32_16x16x32_bf16 v[196:199], v[180:183], v[92:95], v[196:199]\n"
"ds_read_b64_tr_b16 v[180:181], v254 offset:4096\n"
"ds_read_b128 v[120:123], v230 offset:8192\n"
"ds_read_b128 v[124:127], v230 offset:9216\n"
"s_waitcnt lgkmcnt(15)\n"
"v_mfma_f32_16x16x32_bf16 v[192:195], v[184:187], v[96:99], v[192:195]\n"
"ds_read_b64_tr_b16 v[182:183], v254 offset:18432\n"
"v_mfma_f32_16x16x32_bf16 v[196:199], v[184:187], v[100:103], v[196:199]\n"
"ds_read_b64_tr_b16 v[184:185], v254 offset:5120\n"
"ds_read_b128 v[128:131], v230 offset:12288\n"
"ds_read_b128 v[132:135], v230 offset:13312\n"
"v_mfma_f32_16x16x32_bf16 v[192:195], v[188:191], v[104:107], v[192:195]\n"
"ds_read_b64_tr_b16 v[186:187], v254 offset:19456\n"
"v_mfma_f32_16x16x32_bf16 v[196:199], v[188:191], v[108:111], v[196:199]\n"
"ds_read_b64_tr_b16 v[188:189], v254 offset:6144\n"
"ds_read_b64_tr_b16 v[190:191], v254 offset:20480\n"
"ds_read2_b64 v[136:139], v241 offset0:0 offset1:128\n"
"ds_read2_b64 v[140:143], v241 offset0:1 offset1:129\n"
"s_waitcnt lgkmcnt(15)\n"
"v_mfma_f32_16x16x32_bf16 v[200:203], v[164:167], v[144:147], 0\n"
"v_mfma_f32_16x16x32_bf16 v[204:207], v[164:167], v[148:151], 0\n"
"s_waitcnt lgkmcnt(8)\n"
"s_barrier\n"
"ds_read_b64_tr_b16 v[164:165], v252 offset:0\n"
"s_lshl_b32 s60, %2, 3\n"
"s_add_u32 s60, s60, s46\n"
"s_cmp_eq_u32 s60, 2044\n"
"s_cbranch_scc1 O31_%=\n"
"E30_%=:\n"
"v_mfma_f32_16x16x32_bf16 v[200:203], v[168:171], v[152:155], v[200:203]\n"
"ds_read_b64_tr_b16 v[166:167], v252 offset:14336\n"
"v_mfma_f32_16x16x32_bf16 v[204:207], v[168:171], v[156:159], v[204:207]\n"
"ds_read_b64_tr_b16 v[168:169], v252 offset:1024\n"
"s_add_u32 s42, s42, 0x8000\n"
"s_and_b32 s42, s42, 0x1ffff\n"
"v_add_u32 v253, s42, v234\n"
"s_mul_i32 s60, s47, 72\n"
"s_add_u32 s60, s60, s48\n"
"s_lshl_b32 s60, s60, 3\n"
"s_add_u32 s60, s60, 12288\n"
"s_cmp_eq_u32 s46, 4\n"
"s_cselect_b32 s58, s60, 0x40000000\n"
"s_waitcnt lgkmcnt(15)\n"
"v_mfma_f32_16x16x16_bf16 v[200:203], v[172:173], v[160:161], v[200:203]\n"
"ds_read_b64_tr_b16 v[170:171], v252 offset:15360\n"
"v_mfma_f32_16x16x16_bf16 v[204:207], v[172:173], v[162:163], v[204:207]\n"
"ds_read_b64_tr_b16 v[172:173], v252 offset:2048\n"
"v_mov_b32 v242, s50\n"
"v_mov_b32 v243, s51\n"
"v_mfma_f32_16x16x32_bf16 v[192:195], v[176:179], v[112:115], v[192:195]\n"
"ds_read_b64_tr_b16 v[174:175], v252 offset:16384\n"
"v_mfma_f32_16x16x32_bf16 v[196:199], v[176:179], v[116:119], v[196:199]\n"
"ds_read_b64_tr_b16 v[176:177], v252 offset:3072\n"
"v_bfe_u32 v240, v255, 4, 4\n"
"v_lshrrev_b32 v241, 8, v255\n"
"v_lshlrev_b32 v241, 2, v241\n"
"v_sub_u32 v248, v240, v241\n"
"v_cmp_gt_u32 vcc, 4, v248\n"
"v_mov_b32 v240, 0x3f80\n"
"v_mov_b32 v241, 0\n"
"s_waitcnt lgkmcnt(11)\n"
"v_mfma_f32_16x16x32_bf16 v[192:195], v[180:183], v[120:123], v[192:195]\n"
"ds_read_b64_tr_b16 v[178:179], v252 offset:17408\n"
"v_mfma_f32_16x16x32_bf16 v[196:199], v[180:183], v[124:127], v[196:199]\n"
"ds_read_b64_tr_b16 v[180:181], v252 offset:4096\n"
"v_cndmask_b32 v240, 0, v240, vcc\n"
"v_lshlrev_b32 v248, 4, v248\n"
"v_lshlrev_b64 v[240:241], v248, v[240:241]\n"
"s_mov_b64 s[20:21], s[16:17]\n"
"s_mov_b32 s22, 2359296\n"
"s_mov_b32 s23, 0x20000\n"
"v_mfma_f32_16x16x32_bf16 v[192:195], v[184:187], v[128:131], v[192:195]\n"
"ds_read_b64_tr_b16 v[182:183], v252 offset:18432\n"
"v_mfma_f32_16x16x32_bf16 v[196:199], v[184:187], v[132:135], v[196:199]\n"
"ds_read_b64_tr_b16 v[184:185], v252 offset:5120\n"
"v_and_b32 v251, 0x70, v255\n"
"v_lshrrev_b32 v248, 7, v255\n"
"v_lshl_add_u32 v248, v248, 13, v251\n"
"s_sub_u32 s62, 8, s48\n"
"v_and_b32 v245, 0x80, v255\n"
"v_mad_u32_u24 v251, v245, s62, v251\n"
"v_lshrrev_b32 v251, 1, v251\n"
"s_lshr_b32 s60, 0xf0f0, s62\n"
"s_or_b32 s40, s60, 0xff00\n"
"s_lshr_b32 s60, 0x0c0c, s62\n"
"s_and_b32 s66, s60, 0xff\n"
"s_cmp_eq_u32 s46, 4\n"
"s_cselect_b32 s67, 8, 0\n"
"s_waitcnt lgkmcnt(11)\n"
"v_mfma_f32_16x16x32_bf16 v[208:211], v[188:191], v[136:139], v[192:195]\n"
"ds_read_b64_tr_b16 v[186:187], v252 offset:19456\n"
"v_mfma_f32_16x16x32_bf16 v[212:215], v[188:191], v[140:143], v[196:199]\n"
"ds_read_b64_tr_b16 v[188:189], v252 offset:6144\n"
"ds_read_b64_tr_b16 v[190:191], v252 offset:20480\n"
"s_mul_i32 s60, s47, 9\n"
"s_add_u32 s60, s60, s48\n"
"s_lshl_b32 s60, s60, 6\n"
"s_add_u32 s35, s60, 12288\n"
"s_lshl_b32 s41, s48, 13\n"
"s_sub_u32 s60, s55, s41\n"
"s_lshl_b32 s61, s48, 16\n"
"s_add_u32 s44, s60, s61\n"
"s_mul_i32 s61, s46, 896\n"
"s_sub_u32 s60, s60, s61\n"
"s_lshl_b32 s61, s48, 10\n"
"s_add_u32 s60, s60, s61\n"
"s_add_u32 s56, s60, 524288\n"
"s_sub_u32 s41, s41, 8192\n"
"s_and_b32 s41, s41, 0xe000\n"
"s_add_u32 s33, s44, s41\n"
"s_sub_u32 s41, s41, 8192\n"
"s_and_b32 s41, s41, 0xe000\n"
"s_add_u32 s34, s44, s41\n"
"s_waitcnt lgkmcnt(10)\n"
"v_mfma_f32_16x16x32_bf16 v[192:195], v[164:167], v[0:3], 0\n"
"v_mfma_f32_16x16x32_bf16 v[196:199], v[164:167], v[4:7], 0\n"
"s_waitcnt lgkmcnt(4)\n"
"s_mov_b32 s45, 2\n"
"s_barrier\n"
"LG2H_%=:\n"
"ds_read_b64_tr_b16 v[164:165], v253 offset:0\n"
"s_cmp_eq_u32 s45, s67\n"
"s_cbranch_scc0 I32_%=\n"
"s_mov_b64 exec, 0xffff\n"
"buffer_load_dwordx2 v[236:237], v251, s[28:31], s35 offen sc1\n"
"s_mov_b64 exec, -1\n"
"I32_%=:\n"
"s_lshl_b32 s62, s53, 16\n"
"s_add_u32 s62, s62, s55\n"
"v_fma_f32 v208, v208, v246, v200\n"
"v_fma_f32 v209, v209, v246, v201\n"
"v_fma_f32 v210, v210, v246, v202\n"
"v_fma_f32 v211, v211, v246, v203\n"
"v_fma_f32 v212, v212, v247, v204\n"
"v_cvt_pk_bf16_f32 v216, v208, v209\n"
"v_cvt_pk_bf16_f32 v217, v210, v211\n"
"v_fma_f32 v213, v213, v247, v205\n"
"v_fma_f32 v214, v214, v247, v206\n"
"v_fma_f32 v215, v215, v247, v207\n"
"v_cvt_pk_bf16_f32 v218, v212, v213\n"
"v_cvt_pk_bf16_f32 v219, v214, v215\n"
"buffer_store_dwordx4 v[216:219], v255, s[28:31], s62 offen sc1\n"
"v_mfma_f32_16x16x32_bf16 v[192:195], v[168:171], v[8:11], v[192:195]\n"
"ds_read_b64_tr_b16 v[166:167], v253 offset:14336\n"
"v_mfma_f32_16x16x32_bf16 v[196:199], v[168:171], v[12:15], v[196:199]\n"
"ds_read_b64_tr_b16 v[168:169], v253 offset:1024\n"
"s_add_u32 s42, s42, 0x8000\n"
"s_and_b32 s42, s42, 0x1ffff\n"
"v_add_u32 v254, s42, v234\n"
"s_waitcnt lgkmcnt(9)\n"
"v_mfma_f32_16x16x32_bf16 v[192:195], v[172:175], v[16:19], v[192:195]\n"
"ds_read_b64_tr_b16 v[170:171], v253 offset:15360\n"
"v_mfma_f32_16x16x32_bf16 v[196:199], v[172:175], v[20:23], v[196:199]\n"
"ds_read_b64_tr_b16 v[172:173], v253 offset:2048\n"
"v_mfma_f32_16x16x32_bf16 v[192:195], v[176:179], v[24:27], v[192:195]\n"
"ds_read_b64_tr_b16 v[174:175], v253 offset:16384\n"
"v_mfma_f32_16x16x32_bf16 v[196:199], v[176:179], v[28:31], v[196:199]\n"
"ds_read_b64_tr_b16 v[176:177], v253 offset:3072\n"
"s_waitcnt lgkmcnt(9)\n"
"v_mfma_f32_16x16x32_bf16 v[192:195], v[180:183], v[32:35], v[192:195]\n"
"ds_read_b64_tr_b16 v[178:179], v253 offset:17408\n"
"v_mfma_f32_16x16x32_bf16 v[196:199], v[180:183], v[36:39], v[196:199]\n"
"ds_read_b64_tr_b16 v[180:181], v253 offset:4096\n"
"s_mov_b32 s52, s53\n"
"s_mov_b32 s53, s43\n"
"s_lshl_b32 s60, s53, 10\n"
"v_add_u32 v229, s60, v228\n"
"s_add_u32 s43, s48, s45\n"
"s_and_b32 s43, s43, 7\n"
"v_mfma_f32_16x16x32_bf16 v[192:195], v[184:187], v[40:43], v[192:195]\n"
"ds_read_b64_tr_b16 v[182:183], v253 offset:18432\n"
"v_mfma_f32_16x16x32_bf16 v[196:199], v[184:187], v[44:47], v[196:199]\n"
"ds_read_b64_tr_b16 v[184:185], v253 offset:5120\n"
"s_waitcnt lgkmcnt(9)\n"
"v_mfma_f32_16x16x32_bf16 v[192:195], v[188:191], v[48:51], v[192:195]\n"
"ds_read_b64_tr_b16 v[186:187], v253 offset:19456\n"
"v_mfma_f32_16x16x32_bf16 v[196:199], v[188:191], v[52:55], v[196:199]\n"
"ds_read_b64_tr_b16 v[188:189], v253 offset:6144\n"
"ds_read_b64_tr_b16 v[190:191], v253 offset:20480\n"
"v_mfma_f32_16x16x32_bf16 v[192:195], v[164:167], v[56:59], v[192:195]\n"
"v_mfma_f32_16x16x32_bf16 v[196:199], v[164:167], v[60:63], v[196:199]\n"
"s_waitcnt lgkmcnt(4)\n"
"s_barrier\n"
"ds_read_b64_tr_b16 v[164:165], v254 offset:0\n"
"s_waitcnt lgkmcnt(9)\n"
"v_mfma_f32_16x16x32_bf16 v[192:195], v[168:171], v[64:67], v[192:195]\n"
"ds_read_b64_tr_b16 v[166:167], v254 offset:14336\n"
"v_mfma_f32_16x16x32_bf16 v[196:199], v[168:171], v[68:71], v[196:199]\n"
"ds_read_b64_tr_b16 v[168:169], v254 offset:1024\n"
"s_add_u32 s42, s42, 0x8000\n"
"s_and_b32 s42, s42, 0x1ffff\n"
"v_add_u32 v252, s42, v234\n"
"v_mfma_f32_16x16x32_bf16 v[192:195], v[172:175], v[72:75], v[192:195]\n"
"ds_read_b64_tr_b16 v[170:171], v254 offset:15360\n"
"v_mfma_f32_16x16x32_bf16 v[196:199], v[172:175], v[76:79], v[196:199]\n"
"ds_read_b64_tr_b16 v[172:173], v229\n"
"s_waitcnt lgkmcnt(9)\n"
"v_mfma_f32_16x16x32_bf16 v[192:195], v[176:179], v[80:83], v[192:195]\n"
"v_mfma_f32_16x16x32_bf16 v[196:199], v[176:179], v[84:87], v[196:199]\n"
"ds_read_b64_tr_b16 v[176:177], v254 offset:3072\n"
"v_mfma_f32_16x16x32_bf16 v[192:195], v[180:183], v[88:91], v[192:195]\n"
"ds_read_b64_tr_b16 v[178:179], v254 offset:17408\n"
"v_mfma_f32_16x16x32_bf16 v[196:199], v[180:183], v[92:95], v[196:199]\n"
"ds_read_b64_tr_b16 v[180:181], v254 offset:4096\n"
"s_waitcnt lgkmcnt(8)\n"
"v_mfma_f32_16x16x32_bf16 v[192:195], v[184:187], v[96:99], v[192:195]\n"
"ds_read_b64_tr_b16 v[182:183], v254 offset:18432\n"
"v_mfma_f32_16x16x32_bf16 v[196:199], v[184:187], v[100:103], v[196:199]\n"
"ds_read_b64_tr_b16 v[184:185], v254 offset:5120\n"
"v_mfma_f32_16x16x32_bf16 v[192:195], v[188:191], v[104:107], v[192:195]\n"
"ds_read_b64_tr_b16 v[186:187], v254 offset:19456\n"
"v_mfma_f32_16x16x32_bf16 v[196:199], v[188:191], v[108:111], v[196:199]\n"
"ds_read_b64_tr_b16 v[188:189], v254 offset:6144\n"
"ds_read_b64_tr_b16 v[190:191], v254 offset:20480\n"
"s_waitcnt lgkmcnt(9)\n"
"v_mfma_f32_16x16x32_bf16 v[200:203], v[164:167], v[144:147], 0\n"
"v_mfma_f32_16x16x32_bf16 v[204:207], v[164:167], v[148:151], 0\n"
"s_waitcnt vmcnt(0)\n"
"s_cmp_eq_u32 s45, s67\n"
"s_cbranch_scc0 I33_%=\n"
"v_cmp_ne_u64 s[60:61], v[236:237], s[50:51]\n"
"s_nop 1\n"
"s_and_b32 s60, s60, s40\n"
"s_cbranch_scc1 O38_%=\n"
"E37_%=:\n"
"I33_%=:\n"
"s_waitcnt lgkmcnt(4)\n"
"s_barrier\n"
"ds_read_b64_tr_b16 v[164:165], v252 offset:0\n"
"s_lshl_b32 s60, s52, 6\n"
"s_add_u32 s60, s60, s58\n"
"s_mov_b64 exec, 1\n"
"buffer_store_dwordx2 v[242:243], off, s[28:31], s60 sc1\n"
"s_mov_b64 exec, -1\n"
"v_mfma_f32_16x16x32_bf16 v[200:203], v[168:171], v[152:155], v[200:203]\n"
"ds_read_b64_tr_b16 v[166:167], v252 offset:14336\n"
"v_mfma_f32_16x16x32_bf16 v[204:207], v[168:171], v[156:159], v[204:207]\n"
"ds_read_b64_tr_b16 v[168:169], v252 offset:1024\n"
"s_add_u32 s42, s42, 0x8000\n"
"s_and_b32 s42, s42, 0x1ffff\n"
"v_add_u32 v253, s42, v234\n"
"s_cmp_eq_u32 s45, 8\n"
"s_cbranch_scc1 LG3_%=\n"
"s_waitcnt lgkmcnt(9)\n"
"v_mfma_f32_16x16x16_bf16 v[200:203], v[172:173], v[160:161], v[200:203]\n"
"ds_read_b64_tr_b16 v[170:171], v252 offset:15360\n"
"v_mfma_f32_16x16x16_bf16 v[204:207], v[172:173], v[162:163], v[204:207]\n"
"ds_read_b64_tr_b16 v[172:173], v252 offset:2048\n"
"v_mfma_f32_16x16x32_bf16 v[192:195], v[176:179], v[112:115], v[192:195]\n"
"ds_read_b64_tr_b16 v[174:175], v252 offset:16384\n"
"v_mfma_f32_16x16x32_bf16 v[196:199], v[176:179], v[116:119], v[196:199]\n"
"ds_read_b64_tr_b16 v[176:177], v252 offset:3072\n"
"s_waitcnt lgkmcnt(9)\n"
"v_mfma_f32_16x16x32_bf16 v[192:195], v[180:183], v[120:123], v[192:195]\n"
"ds_read_b64_tr_b16 v[178:179], v252 offset:17408\n"
"v_mfma_f32_16x16x32_bf16 v[196:199], v[180:183], v[124:127], v[196:199]\n"
"ds_read_b64_tr_b16 v[180:181], v252 offset:4096\n"
"v_mfma_f32_16x16x32_bf16 v[192:195], v[184:187], v[128:131], v[192:195]\n"
"ds_read_b64_tr_b16 v[182:183], v252 offset:18432\n"
"v_mfma_f32_16x16x32_bf16 v[196:199], v[184:187], v[132:135], v[196:199]\n"
"ds_read_b64_tr_b16 v[184:185], v252 offset:5120\n"
"s_waitcnt lgkmcnt(11)\n"
"v_mfma_f32_16x16x32_bf16 v[208:211], v[188:191], v[136:139], v[192:195]\n"
"ds_read_b64_tr_b16 v[186:187], v252 offset:19456\n"
"v_mfma_f32_16x16x32_bf16 v[212:215], v[188:191], v[140:143], v[196:199]\n"
"ds_read_b64_tr_b16 v[188:189], v252 offset:6144\n"
"ds_read_b64_tr_b16 v[190:191], v252 offset:20480\n"
"s_waitcnt lgkmcnt(10)\n"
"v_mfma_f32_16x16x32_bf16 v[192:195], v[164:167], v[0:3], 0\n"
"v_mfma_f32_16x16x32_bf16 v[196:199], v[164:167], v[4:7], 0\n"
"s_waitcnt lgkmcnt(4)\n"
"s_add_u32 s45, s45, 1\n"
"s_barrier\n"
"s_branch LG2H_%=\n"
"LHBD_%=:\n"
"s_cmp_lg_u32 s48, 3\n"
"s_cbranch_scc1 LHND_%=\n"
"v_lshrrev_b32 v240, 4, v255\n"
"v_and_b32 v240, 48, v240\n"
"v_add_u32 v240, 155648, v240\n"
"s_waitcnt lgkmcnt(0)\n"
"ds_read_b128 v[148:151], v240 offset:0\n"
"ds_read_b128 v[156:159], v240 offset:64\n"
"ds_read_b128 v[164:167], v240 offset:128\n"
"s_waitcnt lgkmcnt(2)\n"
"v_mfma_f32_16x16x32_bf16 v[168:171], v[144:147], v[0:3], 0\n"
"v_mfma_f32_16x16x32_bf16 v[172:175], v[144:147], v[4:7], 0\n"
"ds_read_b64_tr_b16 v[144:145], v253 offset:3072\n"
"v_mfma_f32_16x16x32_bf16 v[176:179], v[148:151], v[0:3], 0\n"
"ds_read_b64_tr_b16 v[146:147], v253 offset:3584\n"
"v_mfma_f32_16x16x32_bf16 v[180:183], v[148:151], v[4:7], 0\n"
"buffer_load_dwordx4 v250, s[24:27], s75 offen offset:1024 lds\n"
"s_waitcnt lgkmcnt(3)\n"
"v_mfma_f32_16x16x32_bf16 v[168:171], v[152:155], v[8:11], v[168:171]\n"
"ds_read_b128 v[148:151], v240 offset:192\n"
"v_mfma_f32_16x16x32_bf16 v[172:175], v[152:155], v[12:15], v[172:175]\n"
"ds_read_b64_tr_b16 v[152:153], v253 offset:4096\n"
"v_mfma_f32_16x16x32_bf16 v[176:179], v[156:159], v[8:11], v[176:179]\n"
"ds_read_b64_tr_b16 v[154:155], v253 offset:4608\n"
"v_mfma_f32_16x16x32_bf16 v[180:183], v[156:159], v[12:15], v[180:183]\n"
"buffer_load_dwordx4 v250, s[24:27], s76 offen offset:2048 lds\n"
"v_mul_f32 v184, s54, v184\n"
"v_mul_f32 v185, s54, v185\n"
"v_mul_f32 v186, s54, v186\n"
"v_mul_f32 v187, s54, v187\n"
"s_waitcnt lgkmcnt(5)\n"
"v_mfma_f32_16x16x32_bf16 v[168:171], v[160:163], v[16:19], v[168:171]\n"
"ds_read_b128 v[156:159], v240 offset:256\n"
"v_mfma_f32_16x16x32_bf16 v[172:175], v[160:163], v[20:23], v[172:175]\n"
"ds_read_b64_tr_b16 v[160:161], v253 offset:5120\n"
"v_mfma_f32_16x16x32_bf16 v[176:179], v[164:167], v[16:19], v[176:179]\n"
"ds_read_b64_tr_b16 v[162:163], v253 offset:5632\n"
"v_mfma_f32_16x16x32_bf16 v[180:183], v[164:167], v[20:23], v[180:183]\n"
"ds_read_b128 v[164:167], v240 offset:320\n"
"buffer_load_dwordx4 v250, s[24:27], s77 offen offset:3072 lds\n"
"v_add_u32 v253, s42, v252\n"
"v_mul_f32 v188, s54, v188\n"
"v_mul_f32 v189, s54, v189\n"
"v_mul_f32 v190, s54, v190\n"
"v_mul_f32 v191, s54, v191\n"
"s_waitcnt lgkmcnt(6)\n"
"v_mfma_f32_16x16x32_bf16 v[168:171], v[144:147], v[24:27], v[168:171]\n"
"v_mfma_f32_16x16x32_bf16 v[172:175], v[144:147], v[28:31], v[172:175]\n"
"s_add_u32 m0, m0, 4096\n"
"s_waitcnt vmcnt(16)\n"
"s_barrier\n"
"ds_read_b64_tr_b16 v[144:145], v253 offset:0\n"
"s_add_u32 s43, s42, 24576\n"
"v_mfma_f32_16x16x32_bf16 v[176:179], v[148:151], v[24:27], v[176:179]\n"
"ds_read_b64_tr_b16 v[146:147], v253 offset:512\n"
"v_mfma_f32_16x16x32_bf16 v[180:183], v[148:151], v[28:31], v[180:183]\n"
"buffer_load_dwordx4 v250, s[24:27], s78 offen lds\n"
"v_cvt_pk_bf16_f32 v208, v184, v185\n"
"v_cvt_pk_bf16_f32 v209, v186, v187\n"
"v_cvt_pk_bf16_f32 v210, v188, v189\n"
"v_cvt_pk_bf16_f32 v211, v190, v191\n"
"s_waitcnt lgkmcnt(5)\n"
"v_mfma_f32_16x16x32_bf16 v[168:171], v[152:155], v[32:35], v[168:171]\n"
"ds_read_b128 v[148:151], v240 offset:384\n"
"s_cmp_eq_u32 s43, 122880\n"
"v_mfma_f32_16x16x32_bf16 v[172:175], v[152:155], v[36:39], v[172:175]\n"
"ds_read_b64_tr_b16 v[152:153], v253 offset:1024\n"
"s_cselect_b32 s43, 0, s43\n"
"v_mfma_f32_16x16x32_bf16 v[176:179], v[156:159], v[32:35], v[176:179]\n"
"ds_read_b64_tr_b16 v[154:155], v253 offset:1536\n"
"v_mfma_f32_16x16x32_bf16 v[180:183], v[156:159], v[36:39], v[180:183]\n"
"buffer_load_dwordx4 v250, s[24:27], s79 offen offset:1024 lds\n"
"v_permlane16_swap_b32 v208, v210\n"
"v_permlane16_swap_b32 v209, v211\n"
"buffer_store_dwordx4 v[208:211], v245, s[28:31], s52 offen sc1\n"
"s_waitcnt lgkmcnt(5)\n"
"v_mfma_f32_16x16x32_bf16 v[168:171], v[160:163], v[40:43], v[168:171]\n"
"ds_read_b128 v[156:159], v240 offset:448\n"
"s_add_u32 m0, s44, s57\n"
"v_mfma_f32_16x16x32_bf16 v[172:175], v[160:163], v[44:47], v[172:175]\n"
"ds_read_b64_tr_b16 v[160:161], v253 offset:2048\n"
"v_mfma_f32_16x16x32_bf16 v[176:179], v[164:167], v[40:43], v[176:179]\n"
"ds_read_b64_tr_b16 v[162:163], v253 offset:2560\n"
"v_mfma_f32_16x16x32_bf16 v[180:183], v[164:167], v[44:47], v[180:183]\n"
"ds_read_b128 v[164:167], v240 offset:512\n"
"buffer_load_dwordx4 v250, s[24:27], s80 offen lds\n"
"v_mul_f32 v192, s54, v192\n"
"v_mul_f32 v193, s54, v193\n"
"v_mul_f32 v194, s54, v194\n"
"v_mul_f32 v195, s54, v195\n"
"s_waitcnt lgkmcnt(6)\n"
"v_mfma_f32_16x16x32_bf16 v[168:171], v[144:147], v[48:51], v[168:171]\n"
"v_mfma_f32_16x16x32_bf16 v[172:175], v[144:147], v[52:55], v[172:175]\n"
"ds_read_b64_tr_b16 v[144:145], v253 offset:3072\n"
"v_mfma_f32_16x16x32_bf16 v[176:179], v[148:151], v[48:51], v[176:179]\n"
"ds_read_b64_tr_b16 v[146:147], v253 offset:3584\n"
"v_mfma_f32_16x16x32_bf16 v[180:183], v[148:151], v[52:55], v[180:183]\n"
"buffer_load_dwordx4 v250, s[24:27], s81 offen offset:1024 lds\n"
"v_mul_f32 v196, s54, v196\n"
"v_mul_f32 v197, s54, v197\n"
"v_mul_f32 v198, s54, v198\n"
"v_mul_f32 v199, s54, v199\n"
"s_waitcnt lgkmcnt(5)\n"
"v_mfma_f32_16x16x32_bf16 v[168:171], v[152:155], v[56:59], v[168:171]\n"
"ds_read_b128 v[148:151], v240 offset:576\n"
"v_mfma_f32_16x16x32_bf16 v[172:175], v[152:155], v[60:63], v[172:175]\n"
"ds_read_b64_tr_b16 v[152:153], v253 offset:4096\n"
"v_mfma_f32_16x16x32_bf16 v[176:179], v[156:159], v[56:59], v[176:179]\n"
"ds_read_b64_tr_b16 v[154:155], v253 offset:4608\n"
"v_mfma_f32_16x16x32_bf16 v[180:183], v[156:159], v[60:63], v[180:183]\n"
"buffer_load_dwordx4 v250, s[24:27], s82 offen offset:2048 lds\n"
"v_cvt_pk_bf16_f32 v212, v192, v193\n"
"v_cvt_pk_bf16_f32 v213, v194, v195\n"
"v_cvt_pk_bf16_f32 v214, v196, v197\n"
"v_cvt_pk_bf16_f32 v215, v198, v199\n"
"s_waitcnt lgkmcnt(5)\n"
"v_mfma_f32_16x16x32_bf16 v[168:171], v[160:163], v[64:67], v[168:171]\n"
"ds_read_b128 v[156:159], v240 offset:640\n"
"v_mfma_f32_16x16x32_bf16 v[172:175], v[160:163], v[68:71], v[172:175]\n"
"ds_read_b64_tr_b16 v[160:161], v253 offset:5120\n"
"v_mfma_f32_16x16x32_bf16 v[176:179], v[164:167], v[64:67], v[176:179]\n"
"ds_read_b64_tr_b16 v[162:163], v253 offset:5632\n"
"v_mfma_f32_16x16x32_bf16 v[180:183], v[164:167], v[68:71], v[180:183]\n"
"ds_read_b128 v[164:167], v240 offset:704\n"
"buffer_load_dwordx4 v250, s[24:27], s83 offen offset:3072 lds\n"
"v_add_u32 v253, s43, v252\n"
"v_permlane16_swap_b32 v212, v214\n"
"v_permlane16_swap_b32 v213, v215\n"
"buffer_store_dwordx4 v[212:215], v246, s[28:31], s52 offen sc1\n"
"s_waitcnt lgkmcnt(6)\n"
"v_mfma_f32_16x16x32_bf16 v[168:171], v[144:147], v[72:75], v[168:171]\n"
"v_mfma_f32_16x16x32_bf16 v[172:175], v[144:147], v[76:79], v[172:175]\n"
"s_add_u32 m0, m0, 4096\n"
"s_waitcnt vmcnt(18)\n"
"s_barrier\n"
"ds_read_b64_tr_b16 v[144:145], v253 offset:0\n"
"s_add_u32 s44, s43, 24576\n"
"v_mfma_f32_16x16x32_bf16 v[176:179], v[148:151], v[72:75], v[176:179]\n"
"ds_read_b64_tr_b16 v[146:147], v253 offset:512\n"
"v_mfma_f32_16x16x32_bf16 v[180:183], v[148:151], v[76:79], v[180:183]\n"
"buffer_load_dwordx4 v250, s[24:27], s84 offen lds\n"
"v_mov_b32 v245, v247\n"
"v_mov_b32 v246, v248\n"
"s_mov_b32 s52, s53\n"
"s_mov_b32 s54, s55\n"
"s_waitcnt lgkmcnt(5)\n"
"v_mfma_f32_16x16x32_bf16 v[168:171], v[152:155], v[80:83], v[168:171]\n"
"ds_read_b128 v[148:151], v240 offset:768\n"
"s_cmp_eq_u32 s44, 122880\n"
"v_mfma_f32_16x16x32_bf16 v[172:175], v[152:155], v[84:87], v[172:175]\n"
"ds_read_b64_tr_b16 v[152:153], v253 offset:1024\n"
"s_cselect_b32 s44, 0, s44\n"
"v_mfma_f32_16x16x32_bf16 v[176:179], v[156:159], v[80:83], v[176:179]\n"
"ds_read_b64_tr_b16 v[154:155], v253 offset:1536\n"
"v_mfma_f32_16x16x32_bf16 v[180:183], v[156:159], v[84:87], v[180:183]\n"
"buffer_load_dwordx4 v250, s[24:27], s85 offen offset:1024 lds\n"
"v_add_u32 v250, s40, v250\n"
"s_waitcnt lgkmcnt(5)\n"
"v_mfma_f32_16x16x32_bf16 v[168:171], v[160:163], v[88:91], v[168:171]\n"
"ds_read_b128 v[156:159], v240 offset:832\n"
"s_add_u32 m0, s42, s57\n"
"v_mfma_f32_16x16x32_bf16 v[172:175], v[160:163], v[92:95], v[172:175]\n"
"ds_read_b64_tr_b16 v[160:161], v253 offset:2048\n"
"v_mfma_f32_16x16x32_bf16 v[176:179], v[164:167], v[88:91], v[176:179]\n"
"ds_read_b64_tr_b16 v[162:163], v253 offset:2560\n"
"v_mfma_f32_16x16x32_bf16 v[180:183], v[164:167], v[92:95], v[180:183]\n"
"ds_read_b128 v[164:167], v240 offset:896\n"
"buffer_load_dwordx4 v250, s[24:27], s68 offen lds\n"
"s_waitcnt lgkmcnt(6)\n"
"v_mfma_f32_16x16x32_bf16 v[168:171], v[144:147], v[96:99], v[168:171]\n"
"v_mfma_f32_16x16x32_bf16 v[172:175], v[144:147], v[100:103], v[172:175]\n"
"ds_read_b64_tr_b16 v[144:145], v253 offset:3072\n"
"v_mfma_f32_16x16x32_bf16 v[176:179], v[148:151], v[96:99], v[176:179]\n"
"ds_read_b64_tr_b16 v[146:147], v253 offset:3584\n"
"v_mfma_f32_16x16x32_bf16 v[180:183], v[148:151], v[100:103], v[180:183]\n"
"buffer_load_dwordx4 v250, s[24:27], s69 offen offset:1024 lds\n"
"s_waitcnt lgkmcnt(5)\n"
"v_mfma_f32_16x16x32_bf16 v[168:171], v[152:155], v[104:107], v[168:171]\n"
"ds_read_b128 v[148:151], v240 offset:960\n"
"v_mfma_f32_16x16x32_bf16 v[172:175], v[152:155], v[108:111], v[172:175]\n"
"ds_read_b64_tr_b16 v[152:153], v253 offset:4096\n"
"v_mfma_f32_16x16x32_bf16 v[176:179], v[156:159], v[104:107], v[176:179]\n"
"ds_read_b64_tr_b16 v[154:155], v253 offset:4608\n"
"v_mfma_f32_16x16x32_bf16 v[180:183], v[156:159], v[108:111], v[180:183]\n"
"buffer_load_dwordx4 v250, s[24:27], s70 offen offset:2048 lds\n"
"v_xor_b32 v252, s35, v252\n"
"s_waitcnt lgkmcnt(5)\n"
"v_mfma_f32_16x16x32_bf16 v[168:171], v[160:163], v[112:115], v[168:171]\n"
"ds_read_b128 v[156:159], v240 offset:1024\n"
"v_mfma_f32_16x16x32_bf16 v[172:175], v[160:163], v[116:119], v[172:175]\n"
"ds_read_b64_tr_b16 v[160:161], v253 offset:5120\n"
"v_mfma_f32_16x16x32_bf16 v[176:179], v[164:167], v[112:115], v[176:179]\n"
"ds_read_b64_tr_b16 v[162:163], v253 offset:5632\n"
"v_mfma_f32_16x16x32_bf16 v[180:183], v[164:167], v[116:119], v[180:183]\n"
"ds_read_b128 v[164:167], v240 offset:1088\n"
"buffer_load_dwordx4 v250, s[24:27], s71 offen offset:3072 lds\n"
"v_add_u32 v253, s44, v252\n"
"v_xor_b32 v254, 32, v253\n"
"s_waitcnt lgkmcnt(6)\n"
"v_mfma_f32_16x16x32_bf16 v[168:171], v[144:147], v[120:123], v[168:171]\n"
"v_mfma_f32_16x16x32_bf16 v[172:175], v[144:147], v[124:127], v[172:175]\n"
"s_add_u32 m0, m0, 4096\n"
"s_waitcnt vmcnt(18)\n"
"s_barrier\n"
"ds_read_b64_tr_b16 v[144:145], v253 offset:0\n"
"s_add_u32 s42, s44, 24576\n"
"v_mfma_f32_16x16x32_bf16 v[176:179], v[148:151], v[120:123], v[176:179]\n"
"ds_read_b64_tr_b16 v[146:147], v253 offset:512\n"
"v_mfma_f32_16x16x32_bf16 v[180:183], v[148:151], v[124:127], v[180:183]\n"
"ds_read_b64_tr_b16 v[148:149], v254 offset:0\n"
"ds_read_b64_tr_b16 v[150:151], v254 offset:512\n"
"buffer_load_dwordx4 v250, s[24:27], s72 offen lds\n"
"s_waitcnt lgkmcnt(7)\n"
"v_mfma_f32_16x16x32_bf16 v[168:171], v[152:155], v[128:131], v[168:171]\n"
"s_cmp_eq_u32 s42, 122880\n"
"v_mfma_f32_16x16x32_bf16 v[172:175], v[152:155], v[132:135], v[172:175]\n"
"ds_read_b64_tr_b16 v[152:153], v253 offset:1024\n"
"s_cselect_b32 s42, 0, s42\n"
"v_mfma_f32_16x16x32_bf16 v[176:179], v[156:159], v[128:131], v[176:179]\n"
"ds_read_b64_tr_b16 v[154:155], v253 offset:1536\n"
"v_mfma_f32_16x16x32_bf16 v[180:183], v[156:159], v[132:135], v[180:183]\n"
"ds_read_b64_tr_b16 v[156:157], v254 offset:1024\n"
"ds_read_b64_tr_b16 v[158:159], v254 offset:1536\n"
"buffer_load_dwordx4 v250, s[24:27], s73 offen offset:1024 lds\n"
"s_waitcnt lgkmcnt(8)\n"
"v_mfma_f32_16x16x32_bf16 v[184:187], v[160:163], v[136:139], v[168:171]\n"
"s_add_u32 m0, s43, s57\n"
"v_mfma_f32_16x16x32_bf16 v[188:191], v[160:163], v[140:143], v[172:175]\n"
"ds_read_b64_tr_b16 v[160:161], v253 offset:2048\n"
"v_mfma_f32_16x16x32_bf16 v[200:203], v[164:167], v[136:139], v[176:179]\n"
"ds_read_b64_tr_b16 v[162:163], v253 offset:2560\n"
"v_mfma_f32_16x16x32_bf16 v[204:207], v[164:167], v[140:143], v[180:183]\n"
"ds_read_b64_tr_b16 v[164:165], v254 offset:2048\n"
"ds_read_b64_tr_b16 v[166:167], v254 offset:2560\n"
"buffer_load_dwordx4 v250, s[24:27], s74 offen lds\n"
"s_lshl_b32 s60, s47, 9\n"
"s_lshl_b32 s61, s49, 6\n"
"s_add_u32 s60, s60, s61\n"
"s_add_u32 s60, s60, 32768\n"
"s_cmp_eq_u32 s48, 3\n"
"s_cselect_b32 s60, s60, 0x40000000\n"
"s_bitcmp1_b32 s49, 0\n"
"s_cselect_b64 vcc, -1, 0\n"
"v_lshrrev_b32 v240, 2, v255\n"
"v_cndmask_b32 v241, v200, v204, vcc\n"
"v_mul_f32 v241, s56, v241\n"
"v_exp_f32 v242, v241\n"
"v_exp_f32 v243, -v241\n"
"v_add_f32 v242, 1.0, v242\n"
"v_add_f32 v243, 1.0, v243\n"
"v_rcp_f32 v242, v242\n"
"v_rcp_f32 v243, v243\n"
"s_mov_b64 exec, 0xffff\n"
"buffer_store_dword v242, v240, s[28:31], s60 offen sc1\n"
"buffer_store_dword v243, v240, s[28:31], s60 offen offset:256 sc1\n"
"s_mov_b64 exec, -1\n"
"s_mov_b32 s45, 1\n"
"s_branch LQD_%=\n"
"LHBH_%=:\n"
"s_waitcnt lgkmcnt(10)\n"
"v_mfma_f32_16x16x32_bf16 v[168:171], v[144:147], v[0:3], 0\n"
"v_mfma_f32_16x16x32_bf16 v[172:175], v[144:147], v[4:7], 0\n"
"ds_read_b64_tr_b16 v[144:145], v253 offset:3072\n"
"s_waitcnt lgkmcnt(7)\n"
"v_mfma_f32_16x16x32_bf16 v[168:171], v[152:155], v[8:11], v[168:171]\n"
"ds_read_b64_tr_b16 v[146:147], v253 offset:3584\n"
"v_mfma_f32_16x16x32_bf16 v[172:175], v[152:155], v[12:15], v[172:175]\n"
"ds_read_b64_tr_b16 v[152:153], v253 offset:4096\n"
"v_mul_f32 v184, s54, v184\n"
"v_mul_f32 v185, s54, v185\n"
"v_mul_f32 v186, s54, v186\n"
"v_mul_f32 v187, s54, v187\n"
"s_waitcnt lgkmcnt(5)\n"
"v_mfma_f32_16x16x32_bf16 v[168:171], v[160:163], v[16:19], v[168:171]\n"
"ds_read_b64_tr_b16 v[154:155], v253 offset:4608\n"
"v_mfma_f32_16x16x32_bf16 v[172:175], v[160:163], v[20:23], v[172:175]\n"
"ds_read_b64_tr_b16 v[160:161], v253 offset:5120\n"
"ds_read_b64_tr_b16 v[162:163], v253 offset:5632\n"
"v_add_u32 v253, s42, v252\n"
"v_mul_f32 v188, s54, v188\n"
"v_mul_f32 v189, s54, v189\n"
"v_mul_f32 v190, s54, v190\n"
"v_mul_f32 v191, s54, v191\n"
"s_waitcnt lgkmcnt(4)\n"
"v_mfma_f32_16x16x32_bf16 v[168:171], v[144:147], v[24:27], v[168:171]\n"
"v_mfma_f32_16x16x32_bf16 v[172:175], v[144:147], v[28:31], v[172:175]\n"
"s_barrier\n"
"ds_read_b64_tr_b16 v[144:145], v253 offset:0\n"
"s_add_u32 s43, s42, 24576\n"
"v_cvt_pk_bf16_f32 v208, v184, v185\n"
"v_cvt_pk_bf16_f32 v209, v186, v187\n"
"v_cvt_pk_bf16_f32 v210, v188, v189\n"
"v_cvt_pk_bf16_f32 v211, v190, v191\n"
"s_waitcnt lgkmcnt(3)\n"
"v_mfma_f32_16x16x32_bf16 v[168:171], v[152:155], v[32:35], v[168:171]\n"
"ds_read_b64_tr_b16 v[146:147], v253 offset:512\n"
"s_cmp_eq_u32 s43, 122880\n"
"v_mfma_f32_16x16x32_bf16 v[172:175], v[152:155], v[36:39], v[172:175]\n"
"ds_read_b64_tr_b16 v[152:153], v253 offset:1024\n"
"s_cselect_b32 s43, 0, s43\n"
"v_permlane16_swap_b32 v208, v210\n"
"v_permlane16_swap_b32 v209, v211\n"
"buffer_store_dwordx4 v[208:211], v245, s[28:31], s52 offen sc1\n"
"s_waitcnt lgkmcnt(3)\n"
"v_mfma_f32_16x16x32_bf16 v[168:171], v[160:163], v[40:43], v[168:171]\n"
"ds_read_b64_tr_b16 v[154:155], v253 offset:1536\n"
"v_mfma_f32_16x16x32_bf16 v[172:175], v[160:163], v[44:47], v[172:175]\n"
"ds_read_b64_tr_b16 v[160:161], v253 offset:2048\n"
"ds_read_b64_tr_b16 v[162:163], v253 offset:2560\n"
"v_mul_f32 v192, s54, v192\n"
"v_mul_f32 v193, s54, v193\n"
"v_mul_f32 v194, s54, v194\n"
"v_mul_f32 v195, s54, v195\n"
"s_waitcnt lgkmcnt(4)\n"
"v_mfma_f32_16x16x32_bf16 v[168:171], v[144:147], v[48:51], v[168:171]\n"
"v_mfma_f32_16x16x32_bf16 v[172:175], v[144:147], v[52:55], v[172:175]\n"
"ds_read_b64_tr_b16 v[144:145], v253 offset:3072\n"
"v_mul_f32 v196, s54, v196\n"
"v_mul_f32 v197, s54, v197\n"
"v_mul_f32 v198, s54, v198\n"
"v_mul_f32 v199, s54, v199\n"
"s_waitcnt lgkmcnt(3)\n"
"v_mfma_f32_16x16x32_bf16 v[168:171], v[152:155], v[56:59], v[168:171]\n"
"ds_read_b64_tr_b16 v[146:147], v253 offset:3584\n"
"v_mfma_f32_16x16x32_bf16 v[172:175], v[152:155], v[60:63], v[172:175]\n"
"ds_read_b64_tr_b16 v[152:153], v253 offset:4096\n"
"v_cvt_pk_bf16_f32 v212, v192, v193\n"
"v_cvt_pk_bf16_f32 v213, v194, v195\n"
"v_cvt_pk_bf16_f32 v214, v196, v197\n"
"v_cvt_pk_bf16_f32 v215, v198, v199\n"
"s_waitcnt lgkmcnt(3)\n"
"v_mfma_f32_16x16x32_bf16 v[168:171], v[160:163], v[64:67], v[168:171]\n"
"ds_read_b64_tr_b16 v[154:155], v253 offset:4608\n"
"v_mfma_f32_16x16x32_bf16 v[172:175], v[160:163], v[68:71], v[172:175]\n"
"ds_read_b64_tr_b16 v[160:161], v253 offset:5120\n"
"ds_read_b64_tr_b16 v[162:163], v253 offset:5632\n"
"v_add_u32 v253, s43, v252\n"
"v_permlane16_swap_b32 v212, v214\n"
"v_permlane16_swap_b32 v213, v215\n"
"buffer_store_dwordx4 v[212:215], v246, s[28:31], s52 offen sc1\n"
"s_waitcnt lgkmcnt(4)\n"
"v_mfma_f32_16x16x32_bf16 v[168:171], v[144:147], v[72:75], v[168:171]\n"
"v_mfma_f32_16x16x32_bf16 v[172:175], v[144:147], v[76:79], v[172:175]\n"
"s_barrier\n"
"ds_read_b64_tr_b16 v[144:145], v253 offset:0\n"
"s_add_u32 s44, s43, 24576\n"
"v_mov_b32 v245, v247\n"
"v_mov_b32 v246, v248\n"
"s_mov_b32 s52, s53\n"
"s_mov_b32 s54, s55\n"
"s_waitcnt lgkmcnt(3)\n"
"v_mfma_f32_16x16x32_bf16 v[168:171], v[152:155], v[80:83], v[168:171]\n"
"ds_read_b64_tr_b16 v[146:147], v253 offset:512\n"
"s_cmp_eq_u32 s44, 122880\n"
"v_mfma_f32_16x16x32_bf16 v[172:175], v[152:155], v[84:87], v[172:175]\n"
"ds_read_b64_tr_b16 v[152:153], v253 offset:1024\n"
"s_cselect_b32 s44, 0, s44\n"
"s_waitcnt lgkmcnt(3)\n"
"v_mfma_f32_16x16x32_bf16 v[168:171], v[160:163], v[88:91], v[168:171]\n"
"ds_read_b64_tr_b16 v[154:155], v253 offset:1536\n"
"v_mfma_f32_16x16x32_bf16 v[172:175], v[160:163], v[92:95], v[172:175]\n"
"ds_read_b64_tr_b16 v[160:161], v253 offset:2048\n"
"ds_read_b64_tr_b16 v[162:163], v253 offset:2560\n"
"s_waitcnt lgkmcnt(4)\n"
"v_mfma_f32_16x16x32_bf16 v[168:171], v[144:147], v[96:99], v[168:171]\n"
"v_mfma_f32_16x16x32_bf16 v[172:175], v[144:147], v[100:103], v[172:175]\n"
"ds_read_b64_tr_b16 v[144:145], v253 offset:3072\n"
"s_waitcnt lgkmcnt(3)\n"
"v_mfma_f32_16x16x32_bf16 v[168:171], v[152:155], v[104:107], v[168:171]\n"
"ds_read_b64_tr_b16 v[146:147], v253 offset:3584\n"
"v_mfma_f32_16x16x32_bf16 v[172:175], v[152:155], v[108:111], v[172:175]\n"
"ds_read_b64_tr_b16 v[152:153], v253 offset:4096\n"
"v_xor_b32 v252, s35, v252\n"
"s_waitcnt lgkmcnt(3)\n"
"v_mfma_f32_16x16x32_bf16 v[168:171], v[160:163], v[112:115], v[168:171]\n"
"ds_read_b64_tr_b16 v[154:155], v253 offset:4608\n"
"v_mfma_f32_16x16x32_bf16 v[172:175], v[160:163], v[116:119], v[172:175]\n"
"ds_read_b64_tr_b16 v[160:161], v253 offset:5120\n"
"ds_read_b64_tr_b16 v[162:163], v253 offset:5632\n"
"v_add_u32 v253, s44, v252\n"
"v_xor_b32 v254, 32, v253\n"
"s_waitcnt lgkmcnt(4)\n"
"v_mfma_f32_16x16x32_bf16 v[168:171], v[144:147], v[120:123], v[168:171]\n"
"v_mfma_f32_16x16x32_bf16 v[172:175], v[144:147], v[124:127], v[172:175]\n"
"s_barrier\n"
"ds_read_b64_tr_b16 v[144:145], v253 offset:0\n"
"s_add_u32 s42, s44, 24576\n"
"ds_read_b64_tr_b16 v[146:147], v253 offset:512\n"
"ds_read_b64_tr_b16 v[148:149], v254 offset:0\n"
"ds_read_b64_tr_b16 v[150:151], v254 offset:512\n"
"s_waitcnt lgkmcnt(6)\n"
"v_mfma_f32_16x16x32_bf16 v[168:171], v[152:155], v[128:131], v[168:171]\n"
"s_cmp_eq_u32 s42, 122880\n"
"v_mfma_f32_16x16x32_bf16 v[172:175], v[152:155], v[132:135], v[172:175]\n"
"ds_read_b64_tr_b16 v[152:153], v253 offset:1024\n"
"s_cselect_b32 s42, 0, s42\n"
"ds_read_b64_tr_b16 v[154:155], v253 offset:1536\n"
"ds_read_b64_tr_b16 v[156:157], v254 offset:1024\n"
"ds_read_b64_tr_b16 v[158:159], v254 offset:1536\n"
"s_waitcnt lgkmcnt(8)\n"
"v_mfma_f32_16x16x32_bf16 v[184:187], v[160:163], v[136:139], v[168:171]\n"
"v_mfma_f32_16x16x32_bf16 v[188:191], v[160:163], v[140:143], v[172:175]\n"
"ds_read_b64_tr_b16 v[160:161], v253 offset:2048\n"
"ds_read_b64_tr_b16 v[162:163], v253 offset:2560\n"
"ds_read_b64_tr_b16 v[164:165], v254 offset:2048\n"
"ds_read_b64_tr_b16 v[166:167], v254 offset:2560\n"
"s_mov_b32 s45, 1\n"
"s_branch LQH_%=\n"
"LHND_%=:\n"
"s_waitcnt lgkmcnt(10)\n"
"v_mfma_f32_16x16x32_bf16 v[168:171], v[144:147], v[0:3], 0\n"
"v_mfma_f32_16x16x32_bf16 v[172:175], v[144:147], v[4:7], 0\n"
"ds_read_b64_tr_b16 v[144:145], v253 offset:3072\n"
"buffer_load_dwordx4 v250, s[24:27], s75 offen offset:1024 lds\n"
"s_waitcnt lgkmcnt(7)\n"
"v_mfma_f32_16x16x32_bf16 v[168:171], v[152:155], v[8:11], v[168:171]\n"
"ds_read_b64_tr_b16 v[146:147], v253 offset:3584\n"
"v_mfma_f32_16x16x32_bf16 v[172:175], v[152:155], v[12:15], v[172:175]\n"
"ds_read_b64_tr_b16 v[152:153], v253 offset:4096\n"
"buffer_load_dwordx4 v250, s[24:27], s76 offen offset:2048 lds\n"
"v_mul_f32 v184, s54, v184\n"
"v_mul_f32 v185, s54, v185\n"
"v_mul_f32 v186, s54, v186\n"
"v_mul_f32 v187, s54, v187\n"
"s_waitcnt lgkmcnt(5)\n"
"v_mfma_f32_16x16x32_bf16 v[168:171], v[160:163], v[16:19], v[168:171]\n"
"ds_read_b64_tr_b16 v[154:155], v253 offset:4608\n"
"v_mfma_f32_16x16x32_bf16 v[172:175], v[160:163], v[20:23], v[172:175]\n"
"ds_read_b64_tr_b16 v[160:161], v253 offset:5120\n"
"ds_read_b64_tr_b16 v[162:163], v253 offset:5632\n"
"buffer_load_dwordx4 v250, s[24:27], s77 offen offset:3072 lds\n"
"v_add_u32 v253, s42, v252\n"
"v_mul_f32 v188, s54, v188\n"
"v_mul_f32 v189, s54, v189\n"
"v_mul_f32 v190, s54, v190\n"
"v_mul_f32 v191, s54, v191\n"
"s_waitcnt lgkmcnt(4)\n"
"v_mfma_f32_16x16x32_bf16 v[168:171], v[144:147], v[24:27], v[168:171]\n"
"v_mfma_f32_16x16x32_bf16 v[172:175], v[144:147], v[28:31], v[172:175]\n"
"s_add_u32 m0, m0, 4096\n"
"s_waitcnt vmcnt(16)\n"
"s_barrier\n"
"ds_read_b64_tr_b16 v[144:145], v253 offset:0\n"
"s_add_u32 s43, s42, 24576\n"
"buffer_load_dwordx4 v250, s[24:27], s78 offen lds\n"
"v_cvt_pk_bf16_f32 v208, v184, v185\n"
"v_cvt_pk_bf16_f32 v209, v186, v187\n"
"v_cvt_pk_bf16_f32 v210, v188, v189\n"
"v_cvt_pk_bf16_f32 v211, v190, v191\n"
"s_waitcnt lgkmcnt(3)\n"
"v_mfma_f32_16x16x32_bf16 v[168:171], v[152:155], v[32:35], v[168:171]\n"
"ds_read_b64_tr_b16 v[146:147], v253 offset:512\n"
"s_cmp_eq_u32 s43, 122880\n"
"v_mfma_f32_16x16x32_bf16 v[172:175], v[152:155], v[36:39], v[172:175]\n"
"ds_read_b64_tr_b16 v[152:153], v253 offset:1024\n"
"s_cselect_b32 s43, 0, s43\n"
"buffer_load_dwordx4 v250, s[24:27], s79 offen offset:1024 lds\n"
"v_permlane16_swap_b32 v208, v210\n"
"v_permlane16_swap_b32 v209, v211\n"
"buffer_store_dwordx4 v[208:211], v245, s[28:31], s52 offen sc1\n"
"s_waitcnt lgkmcnt(3)\n"
"v_mfma_f32_16x16x32_bf16 v[168:171], v[160:163], v[40:43], v[168:171]\n"
"ds_read_b64_tr_b16 v[154:155], v253 offset:1536\n"
"s_add_u32 m0, s44, s57\n"
"v_mfma_f32_16x16x32_bf16 v[172:175], v[160:163], v[44:47], v[172:175]\n"
"ds_read_b64_tr_b16 v[160:161], v253 offset:2048\n"
"ds_read_b64_tr_b16 v[162:163], v253 offset:2560\n"
"buffer_load_dwordx4 v250, s[24:27], s80 offen lds\n"
"v_mul_f32 v192, s54, v192\n"
"v_mul_f32 v193, s54, v193\n"
"v_mul_f32 v194, s54, v194\n"
"v_mul_f32 v195, s54, v195\n"
"s_waitcnt lgkmcnt(4)\n"
"v_mfma_f32_16x16x32_bf16 v[168:171], v[144:147], v[48:51], v[168:171]\n"
"v_mfma_f32_16x16x32_bf16 v[172:175], v[144:147], v[52:55], v[172:175]\n"
"ds_read_b64_tr_b16 v[144:145], v253 offset:3072\n"
"buffer_load_dwordx4 v250, s[24:27], s81 offen offset:1024 lds\n"
"v_mul_f32 v196, s54, v196\n"
"v_mul_f32 v197, s54, v197\n"
"v_mul_f32 v198, s54, v198\n"
"v_mul_f32 v199, s54, v199\n"
"s_waitcnt lgkmcnt(3)\n"
"v_mfma_f32_16x16x32_bf16 v[168:171], v[152:155], v[56:59], v[168:171]\n"
"ds_read_b64_tr_b16 v[146:147], v253 offset:3584\n"
"v_mfma_f32_16x16x32_bf16 v[172:175], v[152:155], v[60:63], v[172:175]\n"
"ds_read_b64_tr_b16 v[152:153], v253 offset:4096\n"
"buffer_load_dwordx4 v250, s[24:27], s82 offen offset:2048 lds\n"
"v_cvt_pk_bf16_f32 v212, v192, v193\n"
"v_cvt_pk_bf16_f32 v213, v194, v195\n"
"v_cvt_pk_bf16_f32 v214, v196, v197\n"
"v_cvt_pk_bf16_f32 v215, v198, v199\n"
"s_waitcnt lgkmcnt(3)\n"
"v_mfma_f32_16x16x32_bf16 v[168:171], v[160:163], v[64:67], v[168:171]\n"
"ds_read_b64_tr_b16 v[154:155], v253 offset:4608\n"
"v_mfma_f32_16x16x32_bf16 v[172:175], v[160:163], v[68:71], v[172:175]\n"
"ds_read_b64_tr_b16 v[160:161], v253 offset:5120\n"
"ds_read_b64_tr_b16 v[162:163], v253 offset:5632\n"
"buffer_load_dwordx4 v250, s[24:27], s83 offen offset:3072 lds\n"
"v_add_u32 v253, s43, v252\n"
"v_permlane16_swap_b32 v212, v214\n"
"v_permlane16_swap_b32 v213, v215\n"
"buffer_store_dwordx4 v[212:215], v246, s[28:31], s52 offen sc1\n"
"s_waitcnt lgkmcnt(4)\n"
"v_mfma_f32_16x16x32_bf16 v[168:171], v[144:147], v[72:75], v[168:171]\n"
"v_mfma_f32_16x16x32_bf16 v[172:175], v[144:147], v[76:79], v[172:175]\n"
"s_add_u32 m0, m0, 4096\n"
"s_waitcnt vmcnt(18)\n"
"s_barrier\n"
"ds_read_b64_tr_b16 v[144:145], v253 offset:0\n"
"s_add_u32 s44, s43, 24576\n"
"buffer_load_dwordx4 v250, s[24:27], s84 offen lds\n"
"v_mov_b32 v245, v247\n"
"v_mov_b32 v246, v248\n"
"s_mov_b32 s52, s53\n"
"s_mov_b32 s54, s55\n"
"s_waitcnt lgkmcnt(3)\n"
"v_mfma_f32_16x16x32_bf16 v[168:171], v[152:155], v[80:83], v[168:171]\n"
"ds_read_b64_tr_b16 v[146:147], v253 offset:512\n"
"s_cmp_eq_u32 s44, 122880\n"
"v_mfma_f32_16x16x32_bf16 v[172:175], v[152:155], v[84:87], v[172:175]\n"
"ds_read_b64_tr_b16 v[152:153], v253 offset:1024\n"
"s_cselect_b32 s44, 0, s44\n"
"buffer_load_dwordx4 v250, s[24:27], s85 offen offset:1024 lds\n"
"v_add_u32 v250, s40, v250\n"
"s_waitcnt lgkmcnt(3)\n"
"v_mfma_f32_16x16x32_bf16 v[168:171], v[160:163], v[88:91], v[168:171]\n"
"ds_read_b64_tr_b16 v[154:155], v253 offset:1536\n"
"s_add_u32 m0, s42, s57\n"
"v_mfma_f32_16x16x32_bf16 v[172:175], v[160:163], v[92:95], v[172:175]\n"
"ds_read_b64_tr_b16 v[160:161], v253 offset:2048\n"
"ds_read_b64_tr_b16 v[162:163], v253 offset:2560\n"
"buffer_load_dwordx4 v250, s[24:27], s68 offen lds\n"
"s_waitcnt lgkmcnt(4)\n"
"v_mfma_f32_16x16x32_bf16 v[168:171], v[144:147], v[96:99], v[168:171]\n"
"v_mfma_f32_16x16x32_bf16 v[172:175], v[144:147], v[100:103], v[172:175]\n"
"ds_read_b64_tr_b16 v[144:145], v253 offset:3072\n"
"buffer_load_dwordx4 v250, s[24:27], s69 offen offset:1024 lds\n"
"s_waitcnt lgkmcnt(3)\n"
"v_mfma_f32_16x16x32_bf16 v[168:171], v[152:155], v[104:107], v[168:171]\n"
"ds_read_b64_tr_b16 v[146:147], v253 offset:3584\n"
"v_mfma_f32_16x16x32_bf16 v[172:175], v[152:155], v[108:111], v[172:175]\n"
"ds_read_b64_tr_b16 v[152:153], v253 offset:4096\n"
"buffer_load_dwordx4 v250, s[24:27], s70 offen offset:2048 lds\n"
"v_xor_b32 v252, s35, v252\n"
"s_waitcnt lgkmcnt(3)\n"
"v_mfma_f32_16x16x32_bf16 v[168:171], v[160:163], v[112:115], v[168:171]\n"
"ds_read_b64_tr_b16 v[154:155], v253 offset:4608\n"
"v_mfma_f32_16x16x32_bf16 v[172:175], v[160:163], v[116:119], v[172:175]\n"
"ds_read_b64_tr_b16 v[160:161], v253 offset:5120\n"
"ds_read_b64_tr_b16 v[162:163], v253 offset:5632\n"
"buffer_load_dwordx4 v250, s[24:27], s71 offen offset:3072 lds\n"
"v_add_u32 v253, s44, v252\n"
"v_xor_b32 v254, 32, v253\n"
"s_waitcnt lgkmcnt(4)\n"
"v_mfma_f32_16x16x32_bf16 v[168:171], v[144:147], v[120:123], v[168:171]\n"
"v_mfma_f32_16x16x32_bf16 v[172:175], v[144:147], v[124:127], v[172:175]\n"
"s_add_u32 m0, m0, 4096\n"
"s_waitcnt vmcnt(18)\n"
"s_barrier\n"
"ds_read_b64_tr_b16 v[144:145], v253 offset:0\n"
"s_add_u32 s42, s44, 24576\n"
"ds_read_b64_tr_b16 v[146:147], v253 offset:512\n"
"ds_read_b64_tr_b16 v[148:149], v254 offset:0\n"
"ds_read_b64_tr_b16 v[150:151], v254 offset:512\n"
"buffer_load_dwordx4 v250, s[24:27], s72 offen lds\n"
"s_waitcnt lgkmcnt(6)\n"
"v_mfma_f32_16x16x32_bf16 v[168:171], v[152:155], v[128:131], v[168:171]\n"
"s_cmp_eq_u32 s42, 122880\n"
"v_mfma_f32_16x16x32_bf16 v[172:175], v[152:155], v[132:135], v[172:175]\n"
"ds_read_b64_tr_b16 v[152:153], v253 offset:1024\n"
"s_cselect_b32 s42, 0, s42\n"
"ds_read_b64_tr_b16 v[154:155], v253 offset:1536\n"
"ds_read_b64_tr_b16 v[156:157], v254 offset:1024\n"
"ds_read_b64_tr_b16 v[158:159], v254 offset:1536\n"
"buffer_load_dwordx4 v250, s[24:27], s73 offen offset:1024 lds\n"
"s_waitcnt lgkmcnt(8)\n"
"v_mfma_f32_16x16x32_bf16 v[184:187], v[160:163], v[136:139], v[168:171]\n"
"s_add_u32 m0, s43, s57\n"
"v_mfma_f32_16x16x32_bf16 v[188:191], v[160:163], v[140:143], v[172:175]\n"
"ds_read_b64_tr_b16 v[160:161], v253 offset:2048\n"
"ds_read_b64_tr_b16 v[162:163], v253 offset:2560\n"
"ds_read_b64_tr_b16 v[164:165], v254 offset:2048\n"
"ds_read_b64_tr_b16 v[166:167], v254 offset:2560\n"
"buffer_load_dwordx4 v250, s[24:27], s74 offen lds\n"
"s_mov_b32 s45, 1\n"
"s_branch LQD_%=\n"
"LFB_%=:\n"
"s_waitcnt lgkmcnt(0)\n"
"s_lshl_b32 s60, s94, 9\n"
"s_add_u32 s60, s60, 19456\n"
"v_subrev_u32 v120, s60, v224\n"
"v_subrev_u32 v121, s60, v226\n"
"s_mov_b32 exec_lo, 0\n"
"v_mov_b32 v180, 0\n"
"s_mov_b32 exec_lo, -1\n"
"v_bfe_u32 v124, v255, 4, 5\n"
"v_lshrrev_b32 v125, 9, v255\n"
"s_mul_i32 s60, s95, 9\n"
"s_lshr_b32 s60, s60, 1\n"
"s_lshl_b32 s60, s60, 2\n"
"v_add_u32 v122, s60, v124\n"
"v_lshrrev_b32 v123, 3, v122\n"
"v_cmp_gt_u32 vcc, 20, v124\n"
"v_and_b32 v122, 7, v122\n"
"v_lshlrev_b32 v122, 1, v122\n"
"v_cndmask_b32 v123, 9, v123, vcc\n"
"v_lshl_add_u32 v122, v123, 10, v122\n"
"v_xor_b32 v123, v123, v125\n"
"v_and_b32 v123, 3, v123\n"
"v_lshl_add_u32 v122, v123, 6, v122\n"
"v_add_u32 v122, 9216, v122\n"
"v_xor_b32 v123, 0x80, v122\n"
"v_lshlrev_b32 v125, 2, v125\n"
"v_sub_u32 v124, v124, v125\n"
"v_mov_b32 v127, 0xff800000\n"
"v_mov_b32 v128, 0xff800000\n"
"s_lshl_b32 s60, s47, 1\n"
"s_add_u32 s60, s60, s94\n"
"s_min_u32 s96, s60, 8\n"
"s_mov_b32 s97, 0\n"
"s_mov_b32 s64, 0\n"
"v_mov_b32 v184, 0\n"
"v_mov_b32 v185, 0\n"
"v_mov_b32 v186, 0\n"
"v_mov_b32 v187, 0\n"
"v_mov_b32 v188, 0\n"
"v_mov_b32 v189, 0\n"
"v_mov_b32 v190, 0\n"
"v_mov_b32 v191, 0\n"
"v_mov_b32 v192, 0\n"
"v_mov_b32 v193, 0\n"
"v_mov_b32 v194, 0\n"
"v_mov_b32 v195, 0\n"
"v_mov_b32 v196, 0\n"
"v_mov_b32 v197, 0\n"
"v_mov_b32 v198, 0\n"
"v_mov_b32 v199, 0\n"
"v_mov_b32 v200, 0\n"
"v_mov_b32 v201, 0\n"
"v_mov_b32 v202, 0\n"
"v_mov_b32 v203, 0\n"
"v_mov_b32 v204, 0\n"
"v_mov_b32 v205, 0\n"
"v_mov_b32 v206, 0\n"
"v_mov_b32 v207, 0\n"
"v_mov_b32 v208, 0\n"
"v_mov_b32 v209, 0\n"
"v_mov_b32 v210, 0\n"
"v_mov_b32 v211, 0\n"
"v_mov_b32 v212, 0\n"
"v_mov_b32 v213, 0\n"
"v_mov_b32 v214, 0\n"
"v_mov_b32 v215, 0\n"
"B1_%=:\n"
"s_add_u32 s60, s64, 1\n"
"s_sub_u32 s60, s60, s94\n"
"s_lshr_b32 s61, s60, 1\n"
"s_mul_i32 s61, s61, 19456\n"
"s_and_b32 s60, s60, 1\n"
"s_xor_b32 s60, s60, 1\n"
"s_lshl_b32 s60, s60, 9\n"
"s_add_u32 s65, s60, s61\n"
"buffer_load_dwordx4 v[160:163], v120, s[20:23], s65 offen sc1\n"
"buffer_load_dwordx4 v[164:167], v121, s[20:23], s65 offen sc1\n"
"buffer_load_ushort v168, v122, s[20:23], s65 offen offset:0 sc1\n"
"buffer_load_ushort v216, v122, s[20:23], s65 offen offset:16 sc1\n"
"buffer_load_ushort v169, v122, s[20:23], s65 offen offset:32 sc1\n"
"buffer_load_ushort v217, v122, s[20:23], s65 offen offset:48 sc1\n"
"buffer_load_ushort v170, v123, s[20:23], s65 offen offset:0 sc1\n"
"buffer_load_ushort v218, v123, s[20:23], s65 offen offset:16 sc1\n"
"buffer_load_ushort v171, v123, s[20:23], s65 offen offset:32 sc1\n"
"buffer_load_ushort v219, v123, s[20:23], s65 offen offset:48 sc1\n"
"buffer_load_ushort v172, v122, s[20:23], s65 offen offset:256 sc1\n"
"buffer_load_ushort v220, v122, s[20:23], s65 offen offset:272 sc1\n"
"buffer_load_ushort v173, v122, s[20:23], s65 offen offset:288 sc1\n"
"buffer_load_ushort v221, v122, s[20:23], s65 offen offset:304 sc1\n"
"buffer_load_ushort v174, v123, s[20:23], s65 offen offset:256 sc1\n"
"buffer_load_ushort v222, v123, s[20:23], s65 offen offset:272 sc1\n"
"buffer_load_ushort v175, v123, s[20:23], s65 offen offset:288 sc1\n"
"buffer_load_ushort v223, v123, s[20:23], s65 offen offset:304 sc1\n"
"s_cmp_eq_u32 s64, 0\n"
"s_cselect_b64 vcc, -1, 0\n"
"v_cndmask_b32 v126, 64, v124, vcc\n"
"s_cmp_eq_u32 s64, 8\n"
"s_cselect_b64 vcc, -1, 0\n"
"v_cndmask_b32 v125, -1, v124, vcc\n"
"s_waitcnt vmcnt(0)\n"
"v_mfma_f32_32x32x16_bf16 v[144:159], v[160:163], v[176:179], v[184:199]\n"
"v_mfma_f32_32x32x16_bf16 v[144:159], v[164:167], v[180:183], v[144:159]\n"
"s_nop 7\n"
"v_lshl_or_b32 v168, v216, 16, v168\n"
"v_lshl_or_b32 v169, v217, 16, v169\n"
"v_lshl_or_b32 v170, v218, 16, v170\n"
"v_lshl_or_b32 v171, v219, 16, v171\n"
"v_lshl_or_b32 v172, v220, 16, v172\n"
"v_lshl_or_b32 v173, v221, 16, v173\n"
"v_lshl_or_b32 v174, v222, 16, v174\n"
"v_lshl_or_b32 v175, v223, 16, v175\n"
"v_cmp_le_i32 vcc, 0, v126\n"
"v_cmp_gt_i32 s[60:61], 0, v125\n"
"s_nop 0\n"
"v_cndmask_b32 v144, v127, v144, vcc\n"
"v_cndmask_b32 v144, v127, v144, s[60:61]\n"
"v_cmp_le_i32 vcc, 1, v126\n"
"v_cmp_gt_i32 s[60:61], 1, v125\n"
"s_nop 0\n"
"v_cndmask_b32 v145, v127, v145, vcc\n"
"v_cndmask_b32 v145, v127, v145, s[60:61]\n"
"v_cmp_le_i32 vcc, 2, v126\n"
"v_cmp_gt_i32 s[60:61], 2, v125\n"
"s_nop 0\n"
"v_cndmask_b32 v146, v127, v146, vcc\n"
"v_cndmask_b32 v146, v127, v146, s[60:61]\n"
"v_cmp_le_i32 vcc, 3, v126\n"
"v_cmp_gt_i32 s[60:61], 3, v125\n"
"s_nop 0\n"
"v_cndmask_b32 v147, v127, v147, vcc\n"
"v_cndmask_b32 v147, v127, v147, s[60:61]\n"
"v_cmp_le_i32 vcc, 8, v126\n"
"v_cmp_gt_i32 s[60:61], 8, v125\n"
"s_nop 0\n"
"v_cndmask_b32 v148, v127, v148, vcc\n"
"v_cndmask_b32 v148, v127, v148, s[60:61]\n"
"v_cmp_le_i32 vcc, 9, v126\n"
"v_cmp_gt_i32 s[60:61], 9, v125\n"
"s_nop 0\n"
"v_cndmask_b32 v149, v127, v149, vcc\n"
"v_cndmask_b32 v149, v127, v149, s[60:61]\n"
"v_cmp_le_i32 vcc, 10, v126\n"
"v_cmp_gt_i32 s[60:61], 10, v125\n"
"s_nop 0\n"
"v_cndmask_b32 v150, v127, v150, vcc\n"
"v_cndmask_b32 v150, v127, v150, s[60:61]\n"
"v_cmp_le_i32 vcc, 11, v126\n"
"v_cmp_gt_i32 s[60:61], 11, v125\n"
"s_nop 0\n"
"v_cndmask_b32 v151, v127, v151, vcc\n"
"v_cndmask_b32 v151, v127, v151, s[60:61]\n"
"v_cmp_le_i32 vcc, 16, v126\n"
"v_cmp_gt_i32 s[60:61], 16, v125\n"
"s_nop 0\n"
"v_cndmask_b32 v152, v127, v152, vcc\n"
"v_cndmask_b32 v152, v127, v152, s[60:61]\n"
"v_cmp_le_i32 vcc, 17, v126\n"
"v_cmp_gt_i32 s[60:61], 17, v125\n"
"s_nop 0\n"
"v_cndmask_b32 v153, v127, v153, vcc\n"
"v_cndmask_b32 v153, v127, v153, s[60:61]\n"
"v_cmp_le_i32 vcc, 18, v126\n"
"v_cmp_gt_i32 s[60:61], 18, v125\n"
"s_nop 0\n"
"v_cndmask_b32 v154, v127, v154, vcc\n"
"v_cndmask_b32 v154, v127, v154, s[60:61]\n"
"v_cmp_le_i32 vcc, 19, v126\n"
"v_cmp_gt_i32 s[60:61], 19, v125\n"
"s_nop 0\n"
"v_cndmask_b32 v155, v127, v155, vcc\n"
"v_cndmask_b32 v155, v127, v155, s[60:61]\n"
"v_cmp_le_i32 vcc, 24, v126\n"
"v_cmp_gt_i32 s[60:61], 24, v125\n"
"s_nop 0\n"
"v_cndmask_b32 v156, v127, v156, vcc\n"
"v_cndmask_b32 v156, v127, v156, s[60:61]\n"
"v_cmp_le_i32 vcc, 25, v126\n"
"v_cmp_gt_i32 s[60:61], 25, v125\n"
"s_nop 0\n"
"v_cndmask_b32 v157, v127, v157, vcc\n"
"v_cndmask_b32 v157, v127, v157, s[60:61]\n"
"v_cmp_le_i32 vcc, 26, v126\n"
"v_cmp_gt_i32 s[60:61], 26, v125\n"
"s_nop 0\n"
"v_cndmask_b32 v158, v127, v158, vcc\n"
"v_cndmask_b32 v158, v127, v158, s[60:61]\n"
"v_cmp_le_i32 vcc, 27, v126\n"
"v_cmp_gt_i32 s[60:61], 27, v125\n"
"s_nop 0\n"
"v_cndmask_b32 v159, v127, v159, vcc\n"
"v_cndmask_b32 v159, v127, v159, s[60:61]\n"
"s_cmp_lg_u32 s97, 0\n"
"s_cbranch_scc1 B2_%=\n"
"v_max3_f32 v128, v144, v145, v128\n"
"v_max3_f32 v128, v146, v147, v128\n"
"v_max3_f32 v128, v148, v149, v128\n"
"v_max3_f32 v128, v150, v151, v128\n"
"v_max3_f32 v128, v152, v153, v128\n"
"v_max3_f32 v128, v154, v155, v128\n"
"v_max3_f32 v128, v156, v157, v128\n"
"v_max3_f32 v128, v158, v159, v128\n"
"s_branch B3_%=\n"
"B2_%=:\n"
"v_exp_f32 v144, v144\n"
"v_exp_f32 v145, v145\n"
"v_exp_f32 v146, v146\n"
"v_exp_f32 v147, v147\n"
"v_exp_f32 v148, v148\n"
"v_exp_f32 v149, v149\n"
"v_exp_f32 v150, v150\n"
"v_exp_f32 v151, v151\n"
"v_exp_f32 v152, v152\n"
"v_exp_f32 v153, v153\n"
"v_exp_f32 v154, v154\n"
"v_exp_f32 v155, v155\n"
"v_exp_f32 v156, v156\n"
"v_exp_f32 v157, v157\n"
"v_exp_f32 v158, v158\n"
"v_exp_f32 v159, v159\n"
"v_cvt_pk_bf16_f32 v144, v144, v145\n"
"v_cvt_pk_bf16_f32 v145, v146, v147\n"
"v_cvt_pk_bf16_f32 v146, v148, v149\n"
"v_cvt_pk_bf16_f32 v147, v150, v151\n"
"v_cvt_pk_bf16_f32 v148, v152, v153\n"
"v_cvt_pk_bf16_f32 v149, v154, v155\n"
"v_cvt_pk_bf16_f32 v150, v156, v157\n"
"v_cvt_pk_bf16_f32 v151, v158, v159\n"
"s_nop 1\n"
"v_mfma_f32_32x32x16_bf16 v[200:215], v[168:171], v[144:147], v[200:215]\n"
"v_mfma_f32_32x32x16_bf16 v[200:215], v[172:175], v[148:151], v[200:215]\n"
"s_nop 7\n"
"s_nop 7\n"
"B3_%=:\n"
"s_add_u32 s64, s64, 1\n"
"s_cmp_le_u32 s64, s96\n"
"s_cbranch_scc1 B1_%=\n"
"s_cmp_lg_u32 s97, 0\n"
"s_cbranch_scc1 B4_%=\n"
"v_mov_b32 v126, v128\n"
"s_nop 1\n"
"v_permlane32_swap_b32 v128, v126\n"
"v_max_f32 v128, v128, v126\n"
"v_sub_f32 v184, 0, v128\n"
"v_sub_f32 v185, 0, v128\n"
"v_sub_f32 v186, 0, v128\n"
"v_sub_f32 v187, 0, v128\n"
"v_sub_f32 v188, 0, v128\n"
"v_sub_f32 v189, 0, v128\n"
"v_sub_f32 v190, 0, v128\n"
"v_sub_f32 v191, 0, v128\n"
"v_sub_f32 v192, 0, v128\n"
"v_sub_f32 v193, 0, v128\n"
"v_sub_f32 v194, 0, v128\n"
"v_sub_f32 v195, 0, v128\n"
"v_sub_f32 v196, 0, v128\n"
"v_sub_f32 v197, 0, v128\n"
"v_sub_f32 v198, 0, v128\n"
"v_sub_f32 v199, 0, v128\n"
"s_mov_b32 s97, 1\n"
"s_mov_b32 s64, 0\n"
"s_branch B1_%=\n"
"B4_%=:\n"
"ds_read_b64_tr_b16 v[164:165], v234 offset:0\n"
"ds_read_b64_tr_b16 v[166:167], v234 offset:14336\n"
"ds_read_b64_tr_b16 v[144:145], v234 offset:1024\n"
"ds_read_b64_tr_b16 v[146:147], v234 offset:15360\n"
"ds_read_b64_tr_b16 v[148:149], v234 offset:2048\n"
"ds_read_b64_tr_b16 v[150:151], v234 offset:16384\n"
"ds_read_b64_tr_b16 v[152:153], v234 offset:3072\n"
"ds_read_b64_tr_b16 v[154:155], v234 offset:17408\n"
"ds_read_b64_tr_b16 v[156:157], v234 offset:4096\n"
"ds_read_b64_tr_b16 v[158:159], v234 offset:18432\n"
"ds_read_b64_tr_b16 v[160:161], v234 offset:5120\n"
"ds_read_b64_tr_b16 v[162:163], v234 offset:19456\n"
"s_waitcnt lgkmcnt(0)\n"
"s_cmp_ge_u32 s46, 4\n"
"s_cbranch_scc1 LFRH_%=\n"
"s_branch LFRD_%=\n"
"O6_%=:\n"
"s_mov_b64 s[24:25], s[20:21]\n"
"s_mov_b32 s26, s22\n"
"v_mov_b32 v250, v255\n"
"s_add_u32 s68, s57, 0\n"
"s_add_u32 s69, s57, 0\n"
"s_add_u32 s70, s57, 0\n"
"s_add_u32 s71, s57, 0\n"
"s_add_u32 s72, s57, 4096\n"
"s_add_u32 s73, s57, 4096\n"
"s_add_u32 s74, s57, 24576\n"
"s_branch E5_%=\n"
"O11_%=:\n"
"s_mov_b32 exec_lo, -1\n"
"s_mov_b32 exec_hi, -1\n"
"s_mov_b32 s64, 200000\n"
"PL7_%=:\n"
"buffer_load_dwordx2 v[236:237], off, s[28:31], s100 sc1\n"
"s_waitcnt vmcnt(0)\n"
"PC9_%=:\n"
"v_cmp_ne_u64 s[60:61], v[236:237], s[50:51]\n"
"s_nop 1\n"
"s_cmp_eq_u64 s[60:61], 0\n"
"s_cbranch_scc1 PD8_%=\n"
"s_sleep 1\n"
"s_sub_u32 s64, s64, 1\n"
"s_cbranch_scc0 PL7_%=\n"
"PD8_%=:\n"
"s_mov_b64 exec, -1\n"
"s_branch E10_%=\n"
"O16_%=:\n"
"s_mov_b32 exec_lo, -1\n"
"s_mov_b32 exec_hi, -1\n"
"s_mov_b32 s64, 200000\n"
"PL12_%=:\n"
"buffer_load_dwordx2 v[236:237], off, s[28:31], s100 offset:8 sc1\n"
"s_waitcnt vmcnt(0)\n"
"PC14_%=:\n"
"v_cmp_ne_u64 s[60:61], v[236:237], s[50:51]\n"
"s_nop 1\n"
"s_cmp_eq_u64 s[60:61], 0\n"
"s_cbranch_scc1 PD13_%=\n"
"s_sleep 1\n"
"s_sub_u32 s64, s64, 1\n"
"s_cbranch_scc0 PL12_%=\n"
"PD13_%=:\n"
"s_mov_b64 exec, -1\n"
"s_branch E15_%=\n"
"O22_%=:\n"
"v_mov_b32 v242, s50\n"
"v_mov_b32 v243, s51\n"
"s_mov_b64 exec, 1\n"
"buffer_store_dwordx2 v[242:243], off, s[28:31], s100 sc1\n"
"s_mov_b64 exec, -1\n"
"s_cmp_lg_u32 s46, 4\n"
"s_cbranch_scc1 J17_%=\n"
"s_mov_b32 exec_lo, s66\n"
"s_mov_b32 exec_hi, s67\n"
"s_mov_b32 s64, 200000\n"
"s_branch PC20_%=\n"
"PL18_%=:\n"
"buffer_load_dwordx2 v[236:237], v240, s[28:31], s65 offen sc1\n"
"s_waitcnt vmcnt(0)\n"
"PC20_%=:\n"
"v_cmp_ne_u64 s[60:61], v[236:237], s[50:51]\n"
"s_nop 1\n"
"s_cmp_eq_u64 s[60:61], 0\n"
"s_cbranch_scc1 PD19_%=\n"
"s_sleep 1\n"
"s_sub_u32 s64, s64, 1\n"
"s_cbranch_scc0 PL18_%=\n"
"PD19_%=:\n"
"s_mov_b64 exec, -1\n"
"J17_%=:\n"
"s_branch E21_%=\n"
"O24_%=:\n"
"s_xor_b32 s60, s47, 0\n"
"s_lshl_b32 s60, s60, 3\n"
"s_add_u32 s60, s60, s48\n"
"s_lshl_b32 s60, s60, 3\n"
"s_add_u32 s65, s60, 4096\n"
"v_mov_b32 v242, s50\n"
"v_mov_b32 v243, s51\n"
"s_mov_b64 exec, 1\n"
"buffer_store_dwordx2 v[242:243], off, s[28:31], s65 sc1\n"
"s_mov_b64 exec, -1\n"
"s_branch E23_%=\n"
"O26_%=:\n"
"s_sub_i32 s62, s47, 4\n"
"s_max_i32 s62, s62, 0\n"
"s_and_b32 s62, s62, -2\n"
"s_or_b32 s63, s47, 1\n"
"s_sub_u32 s63, s63, s62\n"
"s_add_u32 s63, s63, 1\n"
"s_lshl_b32 s63, s63, 3\n"
"s_bfm_b64 s[66:67], s63, 0\n"
"s_lshl_b32 s65, s62, 6\n"
"s_add_u32 s65, s65, 4096\n"
"v_lshrrev_b32 v240, 1, v255\n"
"buffer_load_dwordx2 v[236:237], v240, s[28:31], s65 offen sc1\n"
"s_branch E25_%=\n"
"O31_%=:\n"
"s_mov_b32 s65, 4096\n"
"s_mov_b32 exec_lo, -1\n"
"s_mov_b32 exec_hi, -1\n"
"s_mov_b32 s64, 200000\n"
"PL27_%=:\n"
"buffer_load_dwordx4 v[220:223], v255, s[28:31], s65 offen sc1\n"
"buffer_load_dwordx4 v[224:227], v255, s[28:31], s65 offen offset:1024 sc1\n"
"s_waitcnt vmcnt(0)\n"
"PC29_%=:\n"
"v_cmp_ne_u64 s[60:61], v[220:221], s[50:51]\n"
"v_cmp_ne_u64 s[62:63], v[222:223], s[50:51]\n"
"s_nop 1\n"
"s_or_b64 s[60:61], s[60:61], s[62:63]\n"
"v_cmp_ne_u64 s[62:63], v[224:225], s[50:51]\n"
"s_nop 1\n"
"s_or_b64 s[60:61], s[60:61], s[62:63]\n"
"v_cmp_ne_u64 s[62:63], v[226:227], s[50:51]\n"
"s_nop 1\n"
"s_or_b64 s[60:61], s[60:61], s[62:63]\n"
"s_nop 1\n"
"s_cmp_eq_u64 s[60:61], 0\n"
"s_cbranch_scc1 PD28_%=\n"
"s_sleep 1\n"
"s_sub_u32 s64, s64, 1\n"
"s_cbranch_scc0 PL27_%=\n"
"PD28_%=:\n"
"s_mov_b64 exec, -1\n"
"v_mov_b32 v242, s50\n"
"v_mov_b32 v243, s51\n"
"s_mov_b64 exec, 1\n"
"buffer_store_dwordx2 v[242:243], off, s[28:31], 0 sc1\n"
"s_mov_b64 exec, -1\n"
"s_branch E30_%=\n"
"O38_%=:\n"
"s_mov_b32 exec_lo, s40\n"
"s_mov_b32 exec_hi, 0\n"
"s_mov_b32 s64, 200000\n"
"PL34_%=:\n"
"buffer_load_dwordx2 v[236:237], v251, s[28:31], s35 offen sc1\n"
"s_waitcnt vmcnt(0)\n"
"PC36_%=:\n"
"v_cmp_ne_u64 s[60:61], v[236:237], s[50:51]\n"
"s_nop 1\n"
"s_cmp_eq_u64 s[60:61], 0\n"
"s_cbranch_scc1 PD35_%=\n"
"s_sleep 1\n"
"s_sub_u32 s64, s64, 1\n"
"s_cbranch_scc0 PL34_%=\n"
"PD35_%=:\n"
"s_mov_b64 exec, -1\n"
"s_branch E37_%=\n"
"O45_%=:\n"
"s_mov_b32 exec_lo, s66\n"
"s_mov_b32 exec_hi, 0\n"
"s_mov_b32 s64, 200000\n"
"PL41_%=:\n"
"buffer_load_dwordx2 v[236:237], v251, s[28:31], s35 offen sc1\n"
"s_waitcnt vmcnt(0)\n"
"PC43_%=:\n"
"v_cmp_ne_u64 s[60:61], v[236:237], s[50:51]\n"
"s_nop 1\n"
"s_cmp_eq_u64 s[60:61], 0\n"
"s_cbranch_scc1 PD42_%=\n"
"s_sleep 1\n"
"s_sub_u32 s64, s64, 1\n"
"s_cbranch_scc0 PL41_%=\n"
"PD42_%=:\n"
"s_mov_b64 exec, -1\n"
"s_branch E44_%=\n"
"O49_%=:\n"
"s_mov_b32 s63, 200000\n"
"S46_%=:\n"
"buffer_load_dwordx4 v[16:19], v255, s[28:31], s34 offen sc1\n"
"s_waitcnt vmcnt(0)\n"
"v_max3_u32 v40, v16, v17, v18\n"
"v_max_u32 v40, v40, v19\n"
"v_cmp_eq_u32 s[60:61], -1, v40\n"
"s_nop 1\n"
"s_cmp_lg_u64 s[60:61], 0\n"
"s_cbranch_scc0 S47_%=\n"
"s_sub_u32 s63, s63, 1\n"
"s_cbranch_scc0 S46_%=\n"
"S47_%=:\n"
"v_mfma_f32_16x16x16_bf16 v[192:195], v[240:241], v[16:17], v[208:211]\n"
"v_mfma_f32_16x16x16_bf16 v[196:199], v[240:241], v[18:19], v[212:215]\n"
"s_nop 4\n"
"s_branch E48_%=\n"
"LEN_%=:\n"
  : "+{v0}"(tid)
  : "s"(__builtin_amdgcn_kernarg_segment_ptr()), "s"(blockIdx.x)
  : "v1","v2","v3","v4","v5","v6","v7","v8","v9","v10","v11","v12","v13","v14","v15","v16","v17","v18","v19","v20","v21","v22","v23","v24","v25","v26","v27","v28","v29","v30","v31","v32","v33","v34","v35","v36","v37","v38","v39","v40","v41","v42","v43","v44","v45","v46","v47","v48","v49","v50","v51","v52","v53","v54","v55","v56","v57","v58","v59","v60","v61","v62","v63","v64","v65","v66","v67","v68","v69","v70","v71","v72","v73","v74","v75","v76","v77","v78","v79","v80","v81","v82","v83","v84","v85","v86","v87","v88","v89","v90","v91","v92","v93","v94","v95","v96","v97","v98","v99","v100","v101","v102","v103","v104","v105","v106","v107","v108","v109","v110","v111","v112","v113","v114","v115","v116","v117","v118","v119","v120","v121","v122","v123","v124","v125","v126","v127","v128","v129","v130","v131","v132","v133","v134","v135","v136","v137","v138","v139","v140","v141","v142","v143","v144","v145","v146","v147","v148","v149","v150","v151","v152","v153","v154","v155","v156","v157","v158","v159","v160","v161","v162","v163","v164","v165","v166","v167","v168","v169","v170","v171","v172","v173","v174","v175","v176","v177","v178","v179","v180","v181","v182","v183","v184","v185","v186","v187","v188","v189","v190","v191","v192","v193","v194","v195","v196","v197","v198","v199","v200","v201","v202","v203","v204","v205","v206","v207","v208","v209","v210","v211","v212","v213","v214","v215","v216","v217","v218","v219","v220","v221","v222","v223","v224","v225","v226","v227","v228","v229","v230","v231","v232","v233","v234","v235","v236","v237","v238","v239","v240","v241","v242","v243","v244","v245","v246","v247","v248","v249","v250","v251","v252","v253","v254","v255","s4","s5","s6","s7","s8","s9","s10","s11","s12","s13","s14","s15","s16","s17","s18","s19","s20","s21","s22","s23","s24","s25","s26","s27","s28","s29","s30","s31","s33","s34","s35","s36","s37","s38","s39","s40","s41","s42","s43","s44","s45","s46","s47","s48","s49","s50","s51","s52","s53","s54","s55","s56","s57","s58","s59","s60","s61","s62","s63","s64","s65","s66","s67","s68","s69","s70","s71","s72","s73","s74","s75","s76","s77","s78","s79","s80","s81","s82","s83","s84","s85","s86","s87","s88","s89","s90","s91","s92","s93","s94","s95","s96","s97","s98","s99","s100","s101","vcc","scc","memory");
#endif
}
extern "C" void kernel_launch(void* const* d_in, const int* in_sizes, int n_in, void* d_out, int out_size, void* d_ws, size_t ws_size,
                              hipStream_t stream) {
  (void)hipFuncSetAttribute((const void*)fused_k, hipFuncAttributeMaxDynamicSharedMemorySize, 163840);
  fused_k<<<256, 512, 163840, stream>>>(d_in[0], d_in[1], d_in[2], d_in[4], d_in[5], d_in[6], d_out, d_ws);
}
```
